```python
import torch

def _softmax(x, axis=-1):
    return torch.softmax(x, dim=axis)
def _gelu(x):
    return torch.nn.functional.gelu(x, approximate="tanh")
def _silu(x):
    return torch.nn.functional.silu(x)
def _sigmoid(x):
    return torch.sigmoid(x)
def _layernorm(x, axis=-1, eps=1e-5):
    m = x.mean(dim=axis, keepdim=True)
    v = x.var(dim=axis, keepdim=True, unbiased=False)
    return (x - m) / torch.sqrt(v + eps)
def _scatter_add(tgt, idx, src, axis=0):
    return tgt.clone().index_add_(axis, idx, src)
def _segment_sum(src, seg, n):
    out = torch.zeros((n,) + src.shape[1:], dtype=src.dtype)
    return out.index_add_(0, seg, src)
def _pad(x, pw):
    flat = tuple(v for pair in reversed(pw) for v in pair)
    return torch.nn.functional.pad(x, flat)

kernel_name = 'standalone__gqa_block_cached__near_ridge'

def setup_inputs():

    _w = (lambda a: (lambda t: t.to(torch.bfloat16) if t.is_floating_point() else t)(torch.as_tensor(a)))
    B, Sq, D = 16, 32, 576
    Hq, Hkv, Sk, Dh = 32, 16, 32, 128
    assert Hq % Hkv == 0, (Hq, Hkv)
    return {
        "x": _w((lambda *s: torch.randn(*s, dtype=torch.bfloat16) * 0.8)(B, Sq, D)),
        "k_cache": _w((lambda *s: torch.randn(*s, dtype=torch.bfloat16) * 0.8)(B, Hkv, Sk, Dh)),
        "v_cache": _w((lambda *s: torch.randn(*s, dtype=torch.bfloat16) * 0.05)(B, Hkv, Sk, Dh)),
        "wq": _w((lambda *s: torch.randn(*s, dtype=torch.bfloat16) * 0.8)(D, Hq * Dh) * (D ** -0.5)),
        "wo": _w((lambda *s: torch.randn(*s, dtype=torch.bfloat16) * 0.05)(Hq * Dh, D) * ((Hq * Dh) ** -0.5)),
    }

def reference(x, k_cache, v_cache, wq, wo):
    B, Sq, D = x.shape
    Hq, Dh = 32, 128
    Hkv = k_cache.shape[1]
    rep = Hq // Hkv
    q = (lambda x, *d: x.permute(*d))((x @ wq).reshape(B, Sq, Hq, Dh), 0, 2, 1, 3)
    k = (lambda x, n, axis: torch.repeat_interleave(x, n, dim=axis))(k_cache, rep, axis=1)
    v = (lambda x, n, axis: torch.repeat_interleave(x, n, dim=axis))(v_cache, rep, axis=1)
    scores = torch.einsum("bhqd,bhkd->bhqk", q, k) * (Dh ** -0.5)
    p = _softmax(scores, axis=-1)
    o = torch.einsum("bhqk,bhkd->bhqd", p, v)
    return (lambda x, *d: x.permute(*d))(o, 0, 2, 1, 3).reshape(B, Sq, Hq * Dh) @ wo
```

```cpp
#include <hip/hip_runtime.h>
#include <hip/hip_bf16.h>
typedef unsigned short u16;
__device__ __forceinline__ float bf2f(u16 b){ return __uint_as_float(((unsigned)b)<<16); }
__device__ __forceinline__ u16 f2bf(float f){ unsigned u=__float_as_uint(f); u += 0x7fffu + ((u>>16)&1u); return (u16)(u>>16); }
__device__ __forceinline__ float rb(float f){ return bf2f(f2bf(f)); }

__global__ void __launch_bounds__(256) k1(const u16* x, const u16* kc, const u16* vc, const u16* wq, u16* o){
  __shared__ float q[32][129];
  __shared__ float p[32][33];
  int b = blockIdx.x >> 5, h = blockIdx.x & 31, hk = h >> 1;
  int t = threadIdx.x;
  for (int i = t; i < 32*128; i += 256){
    int r = i >> 7, c = i & 127;
    float acc = 0.f;
    for (int k = 0; k < 576; ++k) acc += bf2f(x[(b*32+r)*576+k]) * bf2f(wq[k*4096 + h*128 + c]);
    q[r][c] = rb(acc);
  }
  __syncthreads();
  for (int i = t; i < 32*32; i += 256){
    int r = i >> 5, c = i & 31;
    float acc = 0.f;
    for (int k = 0; k < 128; ++k) acc += q[r][k] * bf2f(kc[((b*16+hk)*32+c)*128+k]);
    p[r][c] = rb(rb(acc) * 0.08838834764831845f);
  }
  __syncthreads();
  if (t < 32){
    float m = -1e30f;
    for (int c = 0; c < 32; ++c) m = fmaxf(m, p[t][c]);
    float s = 0.f;
    for (int c = 0; c < 32; ++c){ float e = __expf(p[t][c]-m); p[t][c] = e; s += e; }
    for (int c = 0; c < 32; ++c) p[t][c] = rb(p[t][c]/s);
  }
  __syncthreads();
  for (int i = t; i < 32*128; i += 256){
    int r = i >> 7, c = i & 127;
    float acc = 0.f;
    for (int k = 0; k < 32; ++k) acc += p[r][k] * bf2f(vc[((b*16+hk)*32+k)*128+c]);
    o[(b*32+r)*4096 + h*128 + c] = f2bf(acc);
  }
}
__global__ void __launch_bounds__(192) k2(const u16* o, const u16* wo, u16* out){
  int r = blockIdx.x / 3, c = (blockIdx.x % 3)*192 + threadIdx.x;
  float acc = 0.f;
  for (int k = 0; k < 4096; ++k) acc += bf2f(o[r*4096+k]) * bf2f(wo[k*576+c]);
  out[r*576+c] = f2bf(acc);
}
extern "C" void kernel_launch(void* const* d_in, const int* in_sizes, int n_in, void* d_out, int out_size, void* d_ws, size_t ws_size, hipStream_t stream){
  const u16* x=(const u16*)d_in[0]; const u16* kc=(const u16*)d_in[1]; const u16* vc=(const u16*)d_in[2];
  const u16* wq=(const u16*)d_in[3]; const u16* wo=(const u16*)d_in[4];
  u16* o=(u16*)d_ws;
  k1<<<512,256,0,stream>>>(x,kc,vc,wq,o);
  k2<<<512*3,192,0,stream>>>(o,wo,(u16*)d_out);
}
```

```cpp
#include <hip/hip_runtime.h>
#include <hip/hip_bf16.h>

typedef unsigned long long u64;
typedef unsigned u32;
typedef u32 v4u __attribute__((ext_vector_type(4)));
typedef u32 v2u __attribute__((ext_vector_type(2)));
typedef short s16x4 __attribute__((ext_vector_type(4)));
typedef short bf16x8 __attribute__((ext_vector_type(8)));
typedef float f32x16 __attribute__((ext_vector_type(16)));
typedef float f32x4 __attribute__((ext_vector_type(4)));
typedef float f32x2 __attribute__((ext_vector_type(2)));
typedef __bf16 b16x2 __attribute__((ext_vector_type(2)));
#define LAS __attribute__((address_space(3)))
#define GAS __attribute__((address_space(1)))
#define DI __device__ __forceinline__
typedef __amdgpu_buffer_rsrc_t rsrc_t;

constexpr u32 PG = 8192;
constexpr u32 SLAB = 32768;
constexpr u32 WS_FLAGS = 256 * SLAB;
constexpr u32 WS_BYTES = WS_FLAGS + 4096;
constexpr u32 POISON = 0xFFFFFFFFu;
constexpr u32 OOB = 0x80000000u;
constexpr int SPIN_CAP = 1 << 16;

#define STAMP(i)

template <bool NT> DI void dma(u32 lds, u32 voff, rsrc_t rs, u32 soff) {
  if constexpr (NT) asm volatile("s_mov_b32 m0, %0\n\ts_nop 0\n\tbuffer_load_dwordx4 %1, %2, %3 offen nt lds" :: "s"(lds), "v"(voff), "s"(rs), "s"(soff) : "memory");
  else              asm volatile("s_mov_b32 m0, %0\n\ts_nop 0\n\tbuffer_load_dwordx4 %1, %2, %3 offen lds" :: "s"(lds), "v"(voff), "s"(rs), "s"(soff) : "memory");
}
DI void st16_sc1(v4u v, u32 voff, rsrc_t rs, u32 soff) {
  asm volatile("buffer_store_dwordx4 %0, %1, %2, %3 offen sc1\n\ts_nop 1" :: "v"(v), "v"(voff), "s"(rs), "s"(soff) : "memory");
}
template <int N> DI void tick() { asm volatile("s_waitcnt vmcnt(%0) lgkmcnt(0)\n\ts_barrier" :: "i"(N) : "memory"); }
DI bf16x8 trd(u32 a0, u32 a1) {
  s16x4 lo = __builtin_amdgcn_ds_read_tr16_b64_v4i16((LAS s16x4*)(size_t)a0);
  s16x4 hi = __builtin_amdgcn_ds_read_tr16_b64_v4i16((LAS s16x4*)(size_t)a1);
  return __builtin_shufflevector(lo, hi, 0, 1, 2, 3, 4, 5, 6, 7);
}
DI bf16x8 rd16(u32 a) { return *(LAS bf16x8*)(size_t)a; }
DI u32 pk(float a, float b) { f32x2 v = {a, b}; return __builtin_bit_cast(u32, __builtin_convertvector(v, b16x2)); }
template <int S> DI v4u pack8(const f32x16& c) {
  return v4u{pk(c[8 * S], c[8 * S + 1]), pk(c[8 * S + 2], c[8 * S + 3]), pk(c[8 * S + 4], c[8 * S + 5]), pk(c[8 * S + 6], c[8 * S + 7])};
}
DI v4u nopoison(v4u v) { return v4u{min(v.x, POISON - 1), min(v.y, POISON - 1), min(v.z, POISON - 1), min(v.w, POISON - 1)}; }
DI u32 max4(v4u v) { return max(max(v.x, v.y), max(v.z, v.w)); }
#define MF32(a, b, c) __builtin_amdgcn_mfma_f32_32x32x16_bf16((a), (b), (c), 0, 0, 0)
#define MF16(a, b, c) __builtin_amdgcn_mfma_f32_16x16x32_bf16((a), (b), (c), 0, 0, 0)
#define BC(v) __builtin_bit_cast(bf16x8, (v))
template <int C> DI float dppf(float v) { return __builtin_bit_cast(float, __builtin_amdgcn_mov_dpp(__builtin_bit_cast(int, v), C, 0xF, 0xF, true)); }
template <int C> DI u32 dppu(u32 v) { return (u32)__builtin_amdgcn_mov_dpp((int)v, C, 0xF, 0xF, true); }

enum {
  S0 = 0, S1 = S0 + 3, CLR = S1 + 3, S2 = CLR + 4, S3 = S2 + 3, S4 = S3 + 3, KK = S4 + 3, S5 = KK + 2,
  S6 = S5 + 3,
  S7 = S6 + 3,
  S8 = S7 + 3,
  FL = S8 + 3, VA = FL + 1,
  VB = VA + 3,
  WA = VB + 3,
  WB = WA + 3,
  WC = WB + 2,
  PS = WC + 5,
  WD = PS + 1,
  PL = WD + 6,
  END = PL + 2
};

__global__ void __launch_bounds__(512, 2)
gqa_fused(const void* __restrict__ x, const void* __restrict__ kc, const void* __restrict__ vc, const void* __restrict__ wq,
          const void* __restrict__ wo, void* __restrict__ out, char* __restrict__ ws
) {
  __shared__ __attribute__((aligned(1024))) char smem[20 * PG];
  const u32 L = (u32)(size_t)(LAS char*)smem;
  const u32 tid = threadIdx.x, lane = tid & 63;
  const u32 w = __builtin_amdgcn_readfirstlane(tid >> 6);
  const u32 b = blockIdx.x, hq = b & 7, bp = b >> 5, i = (b >> 3) & 3, h = 4 * hq + i;
  STAMP(0);
  const rsrc_t rx = __builtin_amdgcn_make_buffer_rsrc((void*)x, 0, 512 * 1152, 0x00020000);
  const rsrc_t rk = __builtin_amdgcn_make_buffer_rsrc((void*)kc, 0, 256 * 8192, 0x00020000);
  const rsrc_t rv = __builtin_amdgcn_make_buffer_rsrc((void*)vc, 0, 256 * 8192, 0x00020000);
  const rsrc_t rq = __builtin_amdgcn_make_buffer_rsrc((void*)wq, 0, 576 * 8192, 0x00020000);
  const rsrc_t ro = __builtin_amdgcn_make_buffer_rsrc((void*)wo, 0, 4096 * 1152, 0x00020000);
  const rsrc_t rw = __builtin_amdgcn_make_buffer_rsrc((void*)ws, 0, WS_BYTES, 0x00020000);
  const rsrc_t ry = __builtin_amdgcn_make_buffer_rsrc(out, 0, 512 * 1152, 0x00020000);

  const u32 me = hq * 4 + i;
  GAS u64* myflag = (GAS u64*)(ws + WS_FLAGS) + (bp * 32 + me) * 2;
  const u64 tag = *(const __attribute__((address_space(4))) u64*)(size_t)myflag + 1;
  asm volatile("" :: "s"(ro), "s"(ry));
  const u32 xr = 8 * w + (lane >> 3);
  const u32 vx = xr * 1152 + 16 * ((lane & 7) ^ ((xr >> 1) & 7));
  const u32 pr = 4 * w + (lane >> 4);
  const u32 pc = (lane & 15) ^ (((pr & 3) << 2) | ((pr >> 2) & 3));
  const u32 vq = pr * 8192 + 16 * pc, vkv = pr * 256 + 16 * pc, vo = pr * 1152 + 16 * pc;
  const u32 vo9 = (pr + 64 * (pc >> 3)) * 1152 + 16 * (pc & 7);
  const u32 Lw = L + 1024 * w;
  u32 r0 = bp + 2 * i; r0 = r0 >= 9 ? r0 - 9 : r0;
  const u32 sx = bp * (64 * 1152), sq = 256 * h;
  auto step = [&](int s, int pg) {
    u32 ks = r0 + s; ks = ks >= 9 ? ks - 9 : ks;
    dma<false>(Lw + pg * PG, vx, rx, sx + ks * 128);
    dma<true>(Lw + (pg + 1) * PG, vq, rq, sq + ks * (64 * 8192));
    dma<true>(Lw + (pg + 2) * PG, vq, rq, sq + ks * (64 * 8192) + 32 * 8192);
  };
  auto ldv = [&](int n, int pg) {
    dma<true>(Lw + pg * PG, vkv, rv, ((2 * bp + (n & 1)) * 16 + 2 * hq + ((n >> 1) ^ (bp >> 2))) * 8192);
  };
  auto ldw = [&](int n, int pg) { dma<true>(Lw + pg * PG, vo, ro, (hq * 512 + 32 * (n ^ (bp << 1))) * 1152 + 256 * i); };
  auto ldn = [&](int n, int pg) { dma<true>(Lw + pg * PG, vo9, ro, (h * 128 + 32 * n) * 1152 + 1024); };

  step(0, 0); step(1, 3);
  {
    const v4u pz = {POISON, POISON, POISON, POISON};
    const u32 vc0 = tid * 16, sl = b * SLAB;
    st16_sc1(pz, vc0, rw, sl); st16_sc1(pz, vc0, rw, sl + 8192); st16_sc1(pz, vc0, rw, sl + 16384);
    st16_sc1(pz, lane < 32 ? w * 512 + lane * 16 : OOB, rw, sl + 24576);
  }
  step(2, 6); step(3, 9); step(4, 12);
  dma<true>(Lw + 15 * PG, vkv, rk, ((2 * bp) * 16 + (h >> 1)) * 8192);
  dma<true>(Lw + 16 * PG, vkv, rk, ((2 * bp + 1) * 16 + (h >> 1)) * 8192);
  step(5, 17);
  STAMP(1);

  const u32 hh = lane >> 5, blk = (lane >> 4) & 1, q4 = (lane & 15) >> 2, p4 = lane & 3, l31 = lane & 31;
  const u32 mt = w >> 2, dh = (w >> 1) & 1, kh = w & 1, ct = w & 3;
  auto tr_addr = [&](u32 c, u32 t, u32 b0, u32 b1) {
    return 256 * (8 * hh + 4 * t + q4) + 16 * ((4 * c + 2 * blk + b0) ^ ((q4 << 2) | (2 * hh + t))) + 8 * b1;
  };
  const u32 aq00 = L + PG + kh * PG + tr_addr(2 * dh, 0, p4 & 1, p4 >> 1), aq01 = L + PG + kh * PG + tr_addr(2 * dh, 1, p4 & 1, p4 >> 1);
  const u32 aq10 = aq00 ^ 64, aq11 = aq01 ^ 64;
  const u32 xrow = 32 * mt + l31;
  const u32 ax0 = L + 128 * xrow + 16 * ((4 * kh + hh) ^ ((xrow >> 1) & 7)), ax1 = ax0 ^ 32;
  f32x16 a0, a1;
#pragma unroll
  for (int n = 0; n < 16; ++n) { a0[n] = 0.f; a1[n] = 0.f; }
  auto p1 = [&](int pg) {
    const u32 o = pg * PG;
    const bf16x8 B0 = rd16(ax0 + o), B1 = rd16(ax1 + o);
    const bf16x8 A00 = trd(aq00 + o, aq01 + o), A10 = trd(aq10 + o, aq11 + o);
    const bf16x8 A01 = trd(aq00 + o + 4096, aq01 + o + 4096), A11 = trd(aq10 + o + 4096, aq11 + o + 4096);
    a0 = MF32(A00, B0, a0); a1 = MF32(A10, B0, a1);
    a0 = MF32(A01, B1, a0); a1 = MF32(A11, B1, a1);
  };
  v4u fl;
  tick<S6 - S1>();                     STAMP(2);                          p1(0);
  tick<S6 - CLR>();  step(6, 0);                                          p1(3);
  tick<S7 - S3>();   step(7, 3);
  if (tid == 0) __hip_atomic_store(myflag + 1, tag, __ATOMIC_RELAXED, __HIP_MEMORY_SCOPE_AGENT);
  p1(6);
  tick<S8 - S4>();   step(8, 6);                                          p1(9);
  tick<FL - KK>();
  const u32 vfl = WS_FLAGS + bp * 512 + l31 * 16;
  asm volatile("buffer_load_dwordx4 %0, %1, %2, 0 offen sc1" : "=v"(fl) : "v"(vfl), "s"(rw) : "memory");
  ldv(0, 9); ldv(1, 10); ldv(2, 11);                                      p1(12);
  tick<VB - S6>();   ldv(3, 12); ldn(0, 13); ldn(1, 14);                  p1(17);
  tick<WA - S7>();   ldw(0, 17); ldw(1, 18); ldw(2, 19);                  p1(0);
  tick<WB - S8>();   ldw(3, 0); ldw(4, 1);                                p1(3);
  tick<WC - FL>();   STAMP(3);                                            p1(6);
  const u32 arow = 256 * l31 + 16 * (hh ^ (((l31 & 3) << 2) | ((l31 >> 2) & 3)));
  {
    const u32 ak = L + (15 + mt) * PG + (arow ^ (128 * dh));
    f32x16 s;
#pragma unroll
    for (int n = 0; n < 16; ++n) s[n] = 0.f;
    s = MF32(rd16(ak), BC(pack8<0>(a0)), s);
    s = MF32(rd16(ak ^ 32), BC(pack8<1>(a0)), s);
    s = MF32(rd16(ak ^ 64), BC(pack8<0>(a1)), s);
    s = MF32(rd16(ak ^ 96), BC(pack8<1>(a1)), s);
    const u32 as = L + 2 * PG + w * 4096 + l31 * 128 + 16 * ((hh << 1) ^ (l31 & 7));
#pragma unroll
    for (int g = 0; g < 4; ++g)
      *(LAS f32x4*)(size_t)(as ^ (16 * (((g >> 1) << 2) | (g & 1)))) = f32x4{s[4 * g], s[4 * g + 1], s[4 * g + 2], s[4 * g + 3]};
  }
  asm volatile("s_waitcnt vmcnt(%1) lgkmcnt(0)\n\ts_barrier" : "+v"(fl) : "i"(WC - WA) : "memory");
  STAMP(4);
  ldw(5, 6); ldw(6, 7); ldw(7, 8); ldw(8, 15); ldw(9, 16);
  _Pragma("nounroll") for (int sp = 0; sp < SPIN_CAP; ++sp) {
    const u64 e = ((u64)fl.y << 32 | fl.x) + 1, f = (u64)fl.w << 32 | fl.z;
    if (__all(e == f)) break;
    asm volatile("s_sleep 2\n\tbuffer_load_dwordx4 %0, %1, %2, 0 offen sc1\n\ts_waitcnt vmcnt(0)" : "=&v"(fl) : "v"(vfl), "s"(rw) : "memory");
  }
  {
    const u32 ql = lane >> 3, sg = lane & 7;
    const u32 ar = L + 2 * PG + mt * 16384 + (8 * ct + ql) * 128 + 16 * (sg ^ ql);
    f32x4 s = *(LAS f32x4*)(size_t)ar + *(LAS f32x4*)(size_t)(ar + 4096) + (*(LAS f32x4*)(size_t)(ar + 8192) + *(LAS f32x4*)(size_t)(ar + 12288));
    float m = fmaxf(fmaxf(s[0], s[1]), fmaxf(s[2], s[3]));
    m = fmaxf(m, dppf<0xB1>(m)); m = fmaxf(m, dppf<0x4E>(m)); m = fmaxf(m, dppf<0x141>(m));
    const float c = 0.08838834764831845f * 1.4426950408889634f;
    const float e0 = __builtin_amdgcn_exp2f((s[0] - m) * c), e1 = __builtin_amdgcn_exp2f((s[1] - m) * c);
    const float e2 = __builtin_amdgcn_exp2f((s[2] - m) * c), e3 = __builtin_amdgcn_exp2f((s[3] - m) * c);
    float z = (e0 + e1) + (e2 + e3);
    z += dppf<0xB1>(z); z += dppf<0x4E>(z); z += dppf<0x141>(z);
    const float r = __builtin_amdgcn_rcpf(z);
    const u32 p0 = min(pk(e0 * r, e1 * r), POISON - 1), p1v = min(pk(e2 * r, e3 * r), POISON - 1);
    const v4u pv = {p0, p1v, dppu<0xB1>(p0), dppu<0xB1>(p1v)};
    const u32 vp = mt * 2048 + (sg >> 2) * 1024 + (((sg >> 1) & 1) * 32 + 8 * ct + ql) * 16;
    st16_sc1(pv, (lane & 1) ? OOB : vp, rw, b * SLAB + 24576);
  }
  STAMP(5);
  bf16x8 vf[2][8];
  {
    const u32 av = L + (9 + mt) * PG + (arow ^ (128 * (bp & 1)));
#pragma unroll
    for (int s = 0; s < 8; ++s) { vf[0][s] = rd16(av ^ (32 * s)); vf[1][s] = rd16((av ^ (32 * s)) + 2 * PG); }
  }
  const u32 aw0 = L + tr_addr(ct, 0, p4 >> 1, p4 & 1), aw1 = L + tr_addr(ct, 1, p4 >> 1, p4 & 1);
  f32x16 zero;
#pragma unroll
  for (int n = 0; n < 16; ++n) zero[n] = 0.f;
  const bool w9 = (ct >> 1) == mt;
  v4u u9[2];
  if (w9) {
    const u32 av = L + (9 + mt + 2 * ((i >> 1) ^ (bp >> 2))) * PG + arow;
    const u32 n0 = (aw0 ^ (128 * mt)) + 13 * PG, n1 = (aw1 ^ (128 * mt)) + 13 * PG;
    f32x16 u = zero;
#pragma unroll
    for (int s = 0; s < 4; ++s) {
      u = MF32(rd16(av ^ (32 * s)), trd(n0 + s * 4096, n1 + s * 4096), u);
      u = MF32(rd16(av ^ (32 * s + 128)), trd((n0 ^ 128) + s * 4096, (n1 ^ 128) + s * 4096), u);
    }
    u9[0] = pack8<0>(u); u9[1] = pack8<1>(u);
  }
  v4u uu[4][2];
  auto vw = [&](int hs, int g0, int g1, int g2, int g3) {
    const int pgs[4] = {g0, g1, g2, g3};
    f32x16 u = zero;
    bf16x8 bw[8];
#pragma unroll
    for (int s = 0; s < 8; ++s) {
      const u32 o = pgs[s >> 1] * PG + (s & 1) * 4096;
      bw[s] = trd(aw0 + o, aw1 + o);
    }
    __builtin_amdgcn_sched_barrier(0);
#pragma unroll
    for (int s = 0; s < 8; ++s) u = MF32(vf[hs >> 1][s], bw[s], u);
    uu[hs][0] = pack8<0>(u); uu[hs][1] = pack8<1>(u);
  };
  tick<WD - WB - 1>();
  STAMP(6);
  ldw(10, 2); ldw(11, 3); ldw(12, 4); ldw(13, 5); ldw(14, 9); ldw(15, 10);
  vw(0, 17, 18, 19, 0);
  tick<PL - WC - 3>();
  vw(1, 1, 6, 7, 8);
  tick<PL - WD - 2>();
  STAMP(7);
  v4u pa, pb;
  const u32 vpl = (w & 1) * 2048 + lane * 16, spl = ((bp << 5) | (((w >> 1) ^ (bp >> 1)) << 3) | hq) * SLAB + 24576;
  asm volatile("buffer_load_dwordx4 %0, %2, %3, %4 offen sc1\n\tbuffer_load_dwordx4 %1, %2, %3, %4 offen offset:1024 sc1" : "=&v"(pa), "=&v"(pb) : "v"(vpl), "s"(rw), "s"(spl) : "memory");
  vw(2, 15, 16, 2, 3);
  tick<END - PL>();
  STAMP(8);
  vw(3, 4, 5, 9, 10);
  asm volatile("s_waitcnt vmcnt(0)" : "+v"(pa), "+v"(pb) :: "memory");
  _Pragma("nounroll") for (int sp = 0; sp < SPIN_CAP; ++sp) {
    if (__all(max(max4(pa), max4(pb)) != POISON)) break;
    asm volatile("s_sleep 1\n\tbuffer_load_dwordx4 %0, %2, %3, %4 offen sc1\n\tbuffer_load_dwordx4 %1, %2, %3, %4 offen offset:1024 sc1\n\ts_waitcnt vmcnt(0)" : "=&v"(pa), "=&v"(pb) : "v"(vpl), "s"(rw), "s"(spl) : "memory");
  }
  *(LAS v4u*)(size_t)(L + 11 * PG + w * 2048 + lane * 16) = pa;
  *(LAS v4u*)(size_t)(L + 11 * PG + w * 2048 + 1024 + lane * 16) = pb;
  tick<0>();
  STAMP(9);
  {
    const u32 ap = L + 11 * PG + mt * 2048 + lane * 16;
    f32x16 o = zero;
#pragma unroll
    for (int hs = 0; hs < 4; ++hs) {
      o = MF32(rd16(ap + hs * 4096), BC(uu[hs][0]), o);
      o = MF32(rd16(ap + hs * 4096 + 1024), BC(uu[hs][1]), o);
    }
    const u32 so = ((bp << 5) | (i << 3) | (mt * 4 + (ct >> 1))) * SLAB + hq * 2048 + (ct & 1) * 1024;
    st16_sc1(nopoison(pack8<0>(o)), lane * 16, rw, so);
    st16_sc1(nopoison(pack8<1>(o)), lane * 16, rw, so + 2 * SLAB);
    if (w9) {
      const u32 ao = ap + (i ^ (bp >> 1)) * 4096;
      f32x16 o9 = MF32(rd16(ao), BC(u9[0]), zero);
      o9 = MF32(rd16(ao + 1024), BC(u9[1]), o9);
      const u32 v9 = ((lane >> 4) << 3) * SLAB + (lane & 15) * 16;
      const u32 s9 = ((bp << 5) | (mt * 4 + (ct & 1))) * SLAB + 16384 + me * 256;
      st16_sc1(nopoison(pack8<0>(o9)), v9, rw, s9);
      st16_sc1(nopoison(pack8<1>(o9)), v9, rw, s9 + 2 * SLAB);
    }
  }
  STAMP(10);
  const u32 col = lane & 15;
  v4u sel;
  {
    const u32 one = (col & 1) ? 0x3F800000u : 0x00003F80u;
    sel = v4u{(col >> 1) == 0 ? one : 0u, (col >> 1) == 1 ? one : 0u, (col >> 1) == 2 ? one : 0u, (col >> 1) == 3 ? one : 0u};
  }
  const u32 rowoff = (col & 3) + 8 * ((col >> 2) & 1);
  const u32 rowbase = 64 * bp + 32 * (hq >> 2) + 16 * ((hq >> 1) & 1);
  asm volatile("s_waitcnt vmcnt(0)" ::: "memory");
  STAMP(11);
  {
    const u32 vg = (lane >> 4) * 2048 + w * 256 + col * 16, sgo = b * SLAB;
    v4u ga, gb;
    _Pragma("nounroll") for (int sp = 0; sp < SPIN_CAP; ++sp) {
      asm volatile("buffer_load_dwordx4 %0, %2, %3, %4 offen sc1\n\tbuffer_load_dwordx4 %1, %2, %3, %5 offen sc1\n\ts_waitcnt vmcnt(0)" : "=&v"(ga), "=&v"(gb) : "v"(vg), "s"(rw), "s"(sgo), "s"(sgo + 8192) : "memory");
      if (__all(max(max4(ga), max4(gb)) != POISON)) break;
      __builtin_amdgcn_s_sleep(1);
    }
    f32x4 d = {0.f, 0.f, 0.f, 0.f};
    d = MF16(BC(ga), BC(sel), d); d = MF16(BC(gb), BC(sel), d);
    const u32 vy = (rowbase + 4 * ((w >> 1) & 1) + rowoff) * 1152 + (128 * i + 64 * (hq & 1) + 32 * (w >> 2) + 16 * (w & 1) + 4 * (lane >> 4)) * 2;
    __builtin_amdgcn_raw_buffer_store_b64(v2u{pk(d[0], d[1]), pk(d[2], d[3])}, ry, col < 8 ? vy : OOB, 0, 0);
  }
  STAMP(12);
  if (w == 3) {
    const u32 vg = lane * 16, sgo = b * SLAB + 16384;
    v4u g[8];
    _Pragma("nounroll") for (int sp = 0; sp < SPIN_CAP; ++sp) {
      asm volatile("buffer_load_dwordx4 %0, %8, %9, %10 offen sc1\n\tbuffer_load_dwordx4 %1, %8, %9, %10 offen offset:1024 sc1\n\t"
                   "buffer_load_dwordx4 %2, %8, %9, %10 offen offset:2048 sc1\n\tbuffer_load_dwordx4 %3, %8, %9, %10 offen offset:3072 sc1\n\t"
                   "buffer_load_dwordx4 %4, %8, %9, %11 offen sc1\n\tbuffer_load_dwordx4 %5, %8, %9, %11 offen offset:1024 sc1\n\t"
                   "buffer_load_dwordx4 %6, %8, %9, %11 offen offset:2048 sc1\n\tbuffer_load_dwordx4 %7, %8, %9, %11 offen offset:3072 sc1\n\t"
                   "s_waitcnt vmcnt(0)"
                   : "=&v"(g[0]), "=&v"(g[1]), "=&v"(g[2]), "=&v"(g[3]), "=&v"(g[4]), "=&v"(g[5]), "=&v"(g[6]), "=&v"(g[7])
                   : "v"(vg), "s"(rw), "s"(sgo), "s"(sgo + 4096) : "memory");
      u32 m = 0;
#pragma unroll
      for (int n = 0; n < 8; ++n) m = max(m, max4(g[n]));
      if (__all(m != POISON)) break;
      __builtin_amdgcn_s_sleep(1);
    }
    f32x4 d = {0.f, 0.f, 0.f, 0.f};
#pragma unroll
    for (int n = 0; n < 8; ++n) d = MF16(BC(g[n]), BC(sel), d);
    const u32 vy = (rowbase + 4 * (i >> 1) + rowoff) * 1152 + (512 + 32 * (hq & 1) + 16 * (i & 1) + 4 * (lane >> 4)) * 2;
    __builtin_amdgcn_raw_buffer_store_b64(v2u{pk(d[0], d[1]), pk(d[2], d[3])}, ry, col < 8 ? vy : OOB, 0, 0);
    if (lane == 0) __hip_atomic_store(myflag, tag, __ATOMIC_RELAXED, __HIP_MEMORY_SCOPE_AGENT);
  }
  STAMP(13);
}

extern "C" void kernel_launch(void* const* d_in, const int* in_sizes, int n_in, void* d_out, int out_size, void* d_ws, size_t ws_size,
                              hipStream_t stream) {
  gqa_fused<<<256, 512, 0, stream>>>(d_in[0], d_in[1], d_in[2], d_in[3], d_in[4], d_out, (char*)d_ws
  );
}
```

```cpp
#define DIAGK 99
#include <hip/hip_runtime.h>
#include <hip/hip_bf16.h>

typedef unsigned long long u64;
typedef unsigned u32;
typedef u32 v4u __attribute__((ext_vector_type(4)));
typedef u32 v2u __attribute__((ext_vector_type(2)));
typedef short s16x4 __attribute__((ext_vector_type(4)));
typedef short bf16x8 __attribute__((ext_vector_type(8)));
typedef float f32x16 __attribute__((ext_vector_type(16)));
typedef float f32x4 __attribute__((ext_vector_type(4)));
typedef float f32x2 __attribute__((ext_vector_type(2)));
typedef __bf16 b16x2 __attribute__((ext_vector_type(2)));
#define LAS __attribute__((address_space(3)))
#define GAS __attribute__((address_space(1)))
#define DI __device__ __forceinline__
typedef __amdgpu_buffer_rsrc_t rsrc_t;

constexpr u32 PG = 8192;
constexpr u32 SLAB = 32768;
constexpr u32 WS_FLAGS = 256 * SLAB;
constexpr u32 WS_BYTES = WS_FLAGS + 4096;
constexpr u32 POISON = 0xFFFFFFFFu;
constexpr u32 OOB = 0x80000000u;
constexpr int SPIN_CAP = 1 << 16;

#define STAMP(i) do { if ((i) == 0) { asm volatile("s_memrealtime %0\n\ts_memtime %1\n\ts_waitcnt lgkmcnt(0)" : "=s"(dg_t0), "=s"(dg_c0) :: "memory"); } else if ((i) == DIAGK) { asm volatile("s_memrealtime %0\n\ts_waitcnt lgkmcnt(0)" : "=s"(dg_tk) :: "memory"); } } while (0)

template <bool NT> DI void dma(u32 lds, u32 voff, rsrc_t rs, u32 soff) {
  if constexpr (NT) asm volatile("s_mov_b32 m0, %0\n\ts_nop 0\n\tbuffer_load_dwordx4 %1, %2, %3 offen nt lds" :: "s"(lds), "v"(voff), "s"(rs), "s"(soff) : "memory");
  else              asm volatile("s_mov_b32 m0, %0\n\ts_nop 0\n\tbuffer_load_dwordx4 %1, %2, %3 offen lds" :: "s"(lds), "v"(voff), "s"(rs), "s"(soff) : "memory");
}
DI void st16_sc1(v4u v, u32 voff, rsrc_t rs, u32 soff) {
  asm volatile("buffer_store_dwordx4 %0, %1, %2, %3 offen sc1\n\ts_nop 1" :: "v"(v), "v"(voff), "s"(rs), "s"(soff) : "memory");
}
template <int N> DI void tick() { asm volatile("s_waitcnt vmcnt(%0) lgkmcnt(0)\n\ts_barrier" :: "i"(N) : "memory"); }
DI bf16x8 trd(u32 a0, u32 a1) {
  s16x4 lo = __builtin_amdgcn_ds_read_tr16_b64_v4i16((LAS s16x4*)(size_t)a0);
  s16x4 hi = __builtin_amdgcn_ds_read_tr16_b64_v4i16((LAS s16x4*)(size_t)a1);
  return __builtin_shufflevector(lo, hi, 0, 1, 2, 3, 4, 5, 6, 7);
}
DI bf16x8 rd16(u32 a) { return *(LAS bf16x8*)(size_t)a; }
DI u32 pk(float a, float b) { f32x2 v = {a, b}; return __builtin_bit_cast(u32, __builtin_convertvector(v, b16x2)); }
template <int S> DI v4u pack8(const f32x16& c) {
  return v4u{pk(c[8 * S], c[8 * S + 1]), pk(c[8 * S + 2], c[8 * S + 3]), pk(c[8 * S + 4], c[8 * S + 5]), pk(c[8 * S + 6], c[8 * S + 7])};
}
DI v4u nopoison(v4u v) { return v4u{min(v.x, POISON - 1), min(v.y, POISON - 1), min(v.z, POISON - 1), min(v.w, POISON - 1)}; }
DI u32 max4(v4u v) { return max(max(v.x, v.y), max(v.z, v.w)); }
#define MF32(a, b, c) __builtin_amdgcn_mfma_f32_32x32x16_bf16((a), (b), (c), 0, 0, 0)
#define MF16(a, b, c) __builtin_amdgcn_mfma_f32_16x16x32_bf16((a), (b), (c), 0, 0, 0)
#define BC(v) __builtin_bit_cast(bf16x8, (v))
template <int C> DI float dppf(float v) { return __builtin_bit_cast(float, __builtin_amdgcn_mov_dpp(__builtin_bit_cast(int, v), C, 0xF, 0xF, true)); }
template <int C> DI u32 dppu(u32 v) { return (u32)__builtin_amdgcn_mov_dpp((int)v, C, 0xF, 0xF, true); }

enum {
  S0 = 0, S1 = S0 + 3, CLR = S1 + 3, S2 = CLR + 4, S3 = S2 + 3, S4 = S3 + 3, KK = S4 + 3, S5 = KK + 2,
  S6 = S5 + 3,
  S7 = S6 + 3,
  S8 = S7 + 3,
  FL = S8 + 3, VA = FL + 1,
  VB = VA + 3,
  WA = VB + 3,
  WB = WA + 3,
  WC = WB + 2,
  PS = WC + 5,
  WD = PS + 1,
  PL = WD + 6,
  END = PL + 2
};

__global__ void __launch_bounds__(512, 2)
gqa_fused(const void* __restrict__ x, const void* __restrict__ kc, const void* __restrict__ vc, const void* __restrict__ wq,
          const void* __restrict__ wo, void* __restrict__ out, char* __restrict__ ws
) {
  __shared__ __attribute__((aligned(1024))) char smem[20 * PG];
  const u32 L = (u32)(size_t)(LAS char*)smem;
  const u32 tid = threadIdx.x, lane = tid & 63;
  const u32 w = __builtin_amdgcn_readfirstlane(tid >> 6);
  const u32 b = blockIdx.x, hq = b & 7, bp = b >> 5, i = (b >> 3) & 3, h = 4 * hq + i;
  u64 dg_t0, dg_c0, dg_tk = 0;
  STAMP(0);
  const rsrc_t rx = __builtin_amdgcn_make_buffer_rsrc((void*)x, 0, 512 * 1152, 0x00020000);
  const rsrc_t rk = __builtin_amdgcn_make_buffer_rsrc((void*)kc, 0, 256 * 8192, 0x00020000);
  const rsrc_t rv = __builtin_amdgcn_make_buffer_rsrc((void*)vc, 0, 256 * 8192, 0x00020000);
  const rsrc_t rq = __builtin_amdgcn_make_buffer_rsrc((void*)wq, 0, 576 * 8192, 0x00020000);
  const rsrc_t ro = __builtin_amdgcn_make_buffer_rsrc((void*)wo, 0, 4096 * 1152, 0x00020000);
  const rsrc_t rw = __builtin_amdgcn_make_buffer_rsrc((void*)ws, 0, WS_BYTES, 0x00020000);
  const rsrc_t ry = __builtin_amdgcn_make_buffer_rsrc(out, 0, 512 * 1152, 0x00020000);

  const u32 me = hq * 4 + i;
  GAS u64* myflag = (GAS u64*)(ws + WS_FLAGS) + (bp * 32 + me) * 2;
  const u64 tag = *(const __attribute__((address_space(4))) u64*)(size_t)myflag + 1;
  asm volatile("" :: "s"(ro), "s"(ry));
  const u32 xr = 8 * w + (lane >> 3);
  const u32 vx = xr * 1152 + 16 * ((lane & 7) ^ ((xr >> 1) & 7));
  const u32 pr = 4 * w + (lane >> 4);
  const u32 pc = (lane & 15) ^ (((pr & 3) << 2) | ((pr >> 2) & 3));
  const u32 vq = pr * 8192 + 16 * pc, vkv = pr * 256 + 16 * pc, vo = pr * 1152 + 16 * pc;
  const u32 vo9 = (pr + 64 * (pc >> 3)) * 1152 + 16 * (pc & 7);
  const u32 Lw = L + 1024 * w;
  u32 r0 = bp + 2 * i; r0 = r0 >= 9 ? r0 - 9 : r0;
  const u32 sx = bp * (64 * 1152), sq = 256 * h;
  auto step = [&](int s, int pg) {
    u32 ks = r0 + s; ks = ks >= 9 ? ks - 9 : ks;
    dma<false>(Lw + pg * PG, vx, rx, sx + ks * 128);
    dma<true>(Lw + (pg + 1) * PG, vq, rq, sq + ks * (64 * 8192));
    dma<true>(Lw + (pg + 2) * PG, vq, rq, sq + ks * (64 * 8192) + 32 * 8192);
  };
  auto ldv = [&](int n, int pg) {
    dma<true>(Lw + pg * PG, vkv, rv, ((2 * bp + (n & 1)) * 16 + 2 * hq + ((n >> 1) ^ (bp >> 2))) * 8192);
  };
  auto ldw = [&](int n, int pg) { dma<true>(Lw + pg * PG, vo, ro, (hq * 512 + 32 * (n ^ (bp << 1))) * 1152 + 256 * i); };
  auto ldn = [&](int n, int pg) { dma<true>(Lw + pg * PG, vo9, ro, (h * 128 + 32 * n) * 1152 + 1024); };

  step(0, 0); step(1, 3);
  {
    const v4u pz = {POISON, POISON, POISON, POISON};
    const u32 vc0 = tid * 16, sl = b * SLAB;
    st16_sc1(pz, vc0, rw, sl); st16_sc1(pz, vc0, rw, sl + 8192); st16_sc1(pz, vc0, rw, sl + 16384);
    st16_sc1(pz, lane < 32 ? w * 512 + lane * 16 : OOB, rw, sl + 24576);
  }
  step(2, 6); step(3, 9); step(4, 12);
  dma<true>(Lw + 15 * PG, vkv, rk, ((2 * bp) * 16 + (h >> 1)) * 8192);
  dma<true>(Lw + 16 * PG, vkv, rk, ((2 * bp + 1) * 16 + (h >> 1)) * 8192);
  step(5, 17);
  STAMP(1);

  const u32 hh = lane >> 5, blk = (lane >> 4) & 1, q4 = (lane & 15) >> 2, p4 = lane & 3, l31 = lane & 31;
  const u32 mt = w >> 2, dh = (w >> 1) & 1, kh = w & 1, ct = w & 3;
  auto tr_addr = [&](u32 c, u32 t, u32 b0, u32 b1) {
    return 256 * (8 * hh + 4 * t + q4) + 16 * ((4 * c + 2 * blk + b0) ^ ((q4 << 2) | (2 * hh + t))) + 8 * b1;
  };
  const u32 aq00 = L + PG + kh * PG + tr_addr(2 * dh, 0, p4 & 1, p4 >> 1), aq01 = L + PG + kh * PG + tr_addr(2 * dh, 1, p4 & 1, p4 >> 1);
  const u32 aq10 = aq00 ^ 64, aq11 = aq01 ^ 64;
  const u32 xrow = 32 * mt + l31;
  const u32 ax0 = L + 128 * xrow + 16 * ((4 * kh + hh) ^ ((xrow >> 1) & 7)), ax1 = ax0 ^ 32;
  f32x16 a0, a1;
#pragma unroll
  for (int n = 0; n < 16; ++n) { a0[n] = 0.f; a1[n] = 0.f; }
  auto p1 = [&](int pg) {
    const u32 o = pg * PG;
    const bf16x8 B0 = rd16(ax0 + o), B1 = rd16(ax1 + o);
    const bf16x8 A00 = trd(aq00 + o, aq01 + o), A10 = trd(aq10 + o, aq11 + o);
    const bf16x8 A01 = trd(aq00 + o + 4096, aq01 + o + 4096), A11 = trd(aq10 + o + 4096, aq11 + o + 4096);
    a0 = MF32(A00, B0, a0); a1 = MF32(A10, B0, a1);
    a0 = MF32(A01, B1, a0); a1 = MF32(A11, B1, a1);
  };
  v4u fl;
  tick<S6 - S1>();                     STAMP(2);                          p1(0);
  tick<S6 - CLR>();  step(6, 0);                                          p1(3);
  tick<S7 - S3>();   step(7, 3);
  if (tid == 0) __hip_atomic_store(myflag + 1, tag, __ATOMIC_RELAXED, __HIP_MEMORY_SCOPE_AGENT);
  p1(6);
  tick<S8 - S4>();   step(8, 6);                                          p1(9);
  tick<FL - KK>();
  const u32 vfl = WS_FLAGS + bp * 512 + l31 * 16;
  asm volatile("buffer_load_dwordx4 %0, %1, %2, 0 offen sc1" : "=v"(fl) : "v"(vfl), "s"(rw) : "memory");
  ldv(0, 9); ldv(1, 10); ldv(2, 11);                                      p1(12);
  tick<VB - S6>();   ldv(3, 12); ldn(0, 13); ldn(1, 14);                  p1(17);
  tick<WA - S7>();   ldw(0, 17); ldw(1, 18); ldw(2, 19);                  p1(0);
  tick<WB - S8>();   ldw(3, 0); ldw(4, 1);                                p1(3);
  tick<WC - FL>();   STAMP(3);                                            p1(6);
  const u32 arow = 256 * l31 + 16 * (hh ^ (((l31 & 3) << 2) | ((l31 >> 2) & 3)));
  {
    const u32 ak = L + (15 + mt) * PG + (arow ^ (128 * dh));
    f32x16 s;
#pragma unroll
    for (int n = 0; n < 16; ++n) s[n] = 0.f;
    s = MF32(rd16(ak), BC(pack8<0>(a0)), s);
    s = MF32(rd16(ak ^ 32), BC(pack8<1>(a0)), s);
    s = MF32(rd16(ak ^ 64), BC(pack8<0>(a1)), s);
    s = MF32(rd16(ak ^ 96), BC(pack8<1>(a1)), s);
    const u32 as = L + 2 * PG + w * 4096 + l31 * 128 + 16 * ((hh << 1) ^ (l31 & 7));
#pragma unroll
    for (int g = 0; g < 4; ++g)
      *(LAS f32x4*)(size_t)(as ^ (16 * (((g >> 1) << 2) | (g & 1)))) = f32x4{s[4 * g], s[4 * g + 1], s[4 * g + 2], s[4 * g + 3]};
  }
  asm volatile("s_waitcnt vmcnt(%1) lgkmcnt(0)\n\ts_barrier" : "+v"(fl) : "i"(WC - WA) : "memory");
  STAMP(4);
  ldw(5, 6); ldw(6, 7); ldw(7, 8); ldw(8, 15); ldw(9, 16);
  _Pragma("nounroll") for (int sp = 0; sp < SPIN_CAP; ++sp) {
    const u64 e = ((u64)fl.y << 32 | fl.x) + 1, f = (u64)fl.w << 32 | fl.z;
    if (__all(e == f)) break;
    asm volatile("s_sleep 2\n\tbuffer_load_dwordx4 %0, %1, %2, 0 offen sc1\n\ts_waitcnt vmcnt(0)" : "=&v"(fl) : "v"(vfl), "s"(rw) : "memory");
  }
  {
    const u32 ql = lane >> 3, sg = lane & 7;
    const u32 ar = L + 2 * PG + mt * 16384 + (8 * ct + ql) * 128 + 16 * (sg ^ ql);
    f32x4 s = *(LAS f32x4*)(size_t)ar + *(LAS f32x4*)(size_t)(ar + 4096) + (*(LAS f32x4*)(size_t)(ar + 8192) + *(LAS f32x4*)(size_t)(ar + 12288));
    float m = fmaxf(fmaxf(s[0], s[1]), fmaxf(s[2], s[3]));
    m = fmaxf(m, dppf<0xB1>(m)); m = fmaxf(m, dppf<0x4E>(m)); m = fmaxf(m, dppf<0x141>(m));
    const float c = 0.08838834764831845f * 1.4426950408889634f;
    const float e0 = __builtin_amdgcn_exp2f((s[0] - m) * c), e1 = __builtin_amdgcn_exp2f((s[1] - m) * c);
    const float e2 = __builtin_amdgcn_exp2f((s[2] - m) * c), e3 = __builtin_amdgcn_exp2f((s[3] - m) * c);
    float z = (e0 + e1) + (e2 + e3);
    z += dppf<0xB1>(z); z += dppf<0x4E>(z); z += dppf<0x141>(z);
    const float r = __builtin_amdgcn_rcpf(z);
    const u32 p0 = min(pk(e0 * r, e1 * r), POISON - 1), p1v = min(pk(e2 * r, e3 * r), POISON - 1);
    const v4u pv = {p0, p1v, dppu<0xB1>(p0), dppu<0xB1>(p1v)};
    const u32 vp = mt * 2048 + (sg >> 2) * 1024 + (((sg >> 1) & 1) * 32 + 8 * ct + ql) * 16;
    st16_sc1(pv, (lane & 1) ? OOB : vp, rw, b * SLAB + 24576);
  }
  STAMP(5);
  bf16x8 vf[2][8];
  {
    const u32 av = L + (9 + mt) * PG + (arow ^ (128 * (bp & 1)));
#pragma unroll
    for (int s = 0; s < 8; ++s) { vf[0][s] = rd16(av ^ (32 * s)); vf[1][s] = rd16((av ^ (32 * s)) + 2 * PG); }
  }
  const u32 aw0 = L + tr_addr(ct, 0, p4 >> 1, p4 & 1), aw1 = L + tr_addr(ct, 1, p4 >> 1, p4 & 1);
  f32x16 zero;
#pragma unroll
  for (int n = 0; n < 16; ++n) zero[n] = 0.f;
  const bool w9 = (ct >> 1) == mt;
  v4u u9[2];
  if (w9) {
    const u32 av = L + (9 + mt + 2 * ((i >> 1) ^ (bp >> 2))) * PG + arow;
    const u32 n0 = (aw0 ^ (128 * mt)) + 13 * PG, n1 = (aw1 ^ (128 * mt)) + 13 * PG;
    f32x16 u = zero;
#pragma unroll
    for (int s = 0; s < 4; ++s) {
      u = MF32(rd16(av ^ (32 * s)), trd(n0 + s * 4096, n1 + s * 4096), u);
      u = MF32(rd16(av ^ (32 * s + 128)), trd((n0 ^ 128) + s * 4096, (n1 ^ 128) + s * 4096), u);
    }
    u9[0] = pack8<0>(u); u9[1] = pack8<1>(u);
  }
  v4u uu[4][2];
  auto vw = [&](int hs, int g0, int g1, int g2, int g3) {
    const int pgs[4] = {g0, g1, g2, g3};
    f32x16 u = zero;
    bf16x8 bw[8];
#pragma unroll
    for (int s = 0; s < 8; ++s) {
      const u32 o = pgs[s >> 1] * PG + (s & 1) * 4096;
      bw[s] = trd(aw0 + o, aw1 + o);
    }
    __builtin_amdgcn_sched_barrier(0);
#pragma unroll
    for (int s = 0; s < 8; ++s) u = MF32(vf[hs >> 1][s], bw[s], u);
    uu[hs][0] = pack8<0>(u); uu[hs][1] = pack8<1>(u);
  };
  tick<WD - WB - 1>();
  STAMP(6);
  ldw(10, 2); ldw(11, 3); ldw(12, 4); ldw(13, 5); ldw(14, 9); ldw(15, 10);
  vw(0, 17, 18, 19, 0);
  tick<PL - WC - 3>();
  vw(1, 1, 6, 7, 8);
  tick<PL - WD - 2>();
  STAMP(7);
  v4u pa, pb;
  const u32 vpl = (w & 1) * 2048 + lane * 16, spl = ((bp << 5) | (((w >> 1) ^ (bp >> 1)) << 3) | hq) * SLAB + 24576;
  asm volatile("buffer_load_dwordx4 %0, %2, %3, %4 offen sc1\n\tbuffer_load_dwordx4 %1, %2, %3, %4 offen offset:1024 sc1" : "=&v"(pa), "=&v"(pb) : "v"(vpl), "s"(rw), "s"(spl) : "memory");
  vw(2, 15, 16, 2, 3);
  tick<END - PL>();
  STAMP(8);
  vw(3, 4, 5, 9, 10);
  asm volatile("s_waitcnt vmcnt(0)" : "+v"(pa), "+v"(pb) :: "memory");
  _Pragma("nounroll") for (int sp = 0; sp < SPIN_CAP; ++sp) {
    if (__all(max(max4(pa), max4(pb)) != POISON)) break;
    asm volatile("s_sleep 1\n\tbuffer_load_dwordx4 %0, %2, %3, %4 offen sc1\n\tbuffer_load_dwordx4 %1, %2, %3, %4 offen offset:1024 sc1\n\ts_waitcnt vmcnt(0)" : "=&v"(pa), "=&v"(pb) : "v"(vpl), "s"(rw), "s"(spl) : "memory");
  }
  *(LAS v4u*)(size_t)(L + 11 * PG + w * 2048 + lane * 16) = pa;
  *(LAS v4u*)(size_t)(L + 11 * PG + w * 2048 + 1024 + lane * 16) = pb;
  tick<0>();
  STAMP(9);
  {
    const u32 ap = L + 11 * PG + mt * 2048 + lane * 16;
    f32x16 o = zero;
#pragma unroll
    for (int hs = 0; hs < 4; ++hs) {
      o = MF32(rd16(ap + hs * 4096), BC(uu[hs][0]), o);
      o = MF32(rd16(ap + hs * 4096 + 1024), BC(uu[hs][1]), o);
    }
    const u32 so = ((bp << 5) | (i << 3) | (mt * 4 + (ct >> 1))) * SLAB + hq * 2048 + (ct & 1) * 1024;
    st16_sc1(nopoison(pack8<0>(o)), lane * 16, rw, so);
    st16_sc1(nopoison(pack8<1>(o)), lane * 16, rw, so + 2 * SLAB);
    if (w9) {
      const u32 ao = ap + (i ^ (bp >> 1)) * 4096;
      f32x16 o9 = MF32(rd16(ao), BC(u9[0]), zero);
      o9 = MF32(rd16(ao + 1024), BC(u9[1]), o9);
      const u32 v9 = ((lane >> 4) << 3) * SLAB + (lane & 15) * 16;
      const u32 s9 = ((bp << 5) | (mt * 4 + (ct & 1))) * SLAB + 16384 + me * 256;
      st16_sc1(nopoison(pack8<0>(o9)), v9, rw, s9);
      st16_sc1(nopoison(pack8<1>(o9)), v9, rw, s9 + 2 * SLAB);
    }
  }
  STAMP(10);
  const u32 col = lane & 15;
  v4u sel;
  {
    const u32 one = (col & 1) ? 0x3F800000u : 0x00003F80u;
    sel = v4u{(col >> 1) == 0 ? one : 0u, (col >> 1) == 1 ? one : 0u, (col >> 1) == 2 ? one : 0u, (col >> 1) == 3 ? one : 0u};
  }
  const u32 rowoff = (col & 3) + 8 * ((col >> 2) & 1);
  const u32 rowbase = 64 * bp + 32 * (hq >> 2) + 16 * ((hq >> 1) & 1);
  asm volatile("s_waitcnt vmcnt(0)" ::: "memory");
  STAMP(11);
  {
    const u32 vg = (lane >> 4) * 2048 + w * 256 + col * 16, sgo = b * SLAB;
    v4u ga, gb;
    _Pragma("nounroll") for (int sp = 0; sp < SPIN_CAP; ++sp) {
      asm volatile("buffer_load_dwordx4 %0, %2, %3, %4 offen sc1\n\tbuffer_load_dwordx4 %1, %2, %3, %5 offen sc1\n\ts_waitcnt vmcnt(0)" : "=&v"(ga), "=&v"(gb) : "v"(vg), "s"(rw), "s"(sgo), "s"(sgo + 8192) : "memory");
      if (__all(max(max4(ga), max4(gb)) != POISON)) break;
      __builtin_amdgcn_s_sleep(1);
    }
    f32x4 d = {0.f, 0.f, 0.f, 0.f};
    d = MF16(BC(ga), BC(sel), d); d = MF16(BC(gb), BC(sel), d);
    const u32 vy = (rowbase + 4 * ((w >> 1) & 1) + rowoff) * 1152 + (128 * i + 64 * (hq & 1) + 32 * (w >> 2) + 16 * (w & 1) + 4 * (lane >> 4)) * 2;
    __builtin_amdgcn_raw_buffer_store_b64(v2u{pk(d[0], d[1]), pk(d[2], d[3])}, ry, col < 8 ? vy : OOB, 0, 16);
  }
  STAMP(12);
  if (w == 3) {
    const u32 vg = lane * 16, sgo = b * SLAB + 16384;
    v4u g[8];
    _Pragma("nounroll") for (int sp = 0; sp < SPIN_CAP; ++sp) {
      asm volatile("buffer_load_dwordx4 %0, %8, %9, %10 offen sc1\n\tbuffer_load_dwordx4 %1, %8, %9, %10 offen offset:1024 sc1\n\t"
                   "buffer_load_dwordx4 %2, %8, %9, %10 offen offset:2048 sc1\n\tbuffer_load_dwordx4 %3, %8, %9, %10 offen offset:3072 sc1\n\t"
                   "buffer_load_dwordx4 %4, %8, %9, %11 offen sc1\n\tbuffer_load_dwordx4 %5, %8, %9, %11 offen offset:1024 sc1\n\t"
                   "buffer_load_dwordx4 %6, %8, %9, %11 offen offset:2048 sc1\n\tbuffer_load_dwordx4 %7, %8, %9, %11 offen offset:3072 sc1\n\t"
                   "s_waitcnt vmcnt(0)"
                   : "=&v"(g[0]), "=&v"(g[1]), "=&v"(g[2]), "=&v"(g[3]), "=&v"(g[4]), "=&v"(g[5]), "=&v"(g[6]), "=&v"(g[7])
                   : "v"(vg), "s"(rw), "s"(sgo), "s"(sgo + 4096) : "memory");
      u32 m = 0;
#pragma unroll
      for (int n = 0; n < 8; ++n) m = max(m, max4(g[n]));
      if (__all(m != POISON)) break;
      __builtin_amdgcn_s_sleep(1);
    }
    f32x4 d = {0.f, 0.f, 0.f, 0.f};
#pragma unroll
    for (int n = 0; n < 8; ++n) d = MF16(BC(g[n]), BC(sel), d);
    const u32 vy = (rowbase + 4 * (i >> 1) + rowoff) * 1152 + (512 + 32 * (hq & 1) + 16 * (i & 1) + 4 * (lane >> 4)) * 2;
    __builtin_amdgcn_raw_buffer_store_b64(v2u{pk(d[0], d[1]), pk(d[2], d[3])}, ry, col < 8 ? vy : OOB, 0, 16);
    if (lane == 0) __hip_atomic_store(myflag, tag, __ATOMIC_RELAXED, __HIP_MEMORY_SCOPE_AGENT);
  }
  STAMP(13);
  { u64 dg_t1, dg_c1; asm volatile("s_memrealtime %0\n\ts_memtime %1\n\ts_waitcnt lgkmcnt(0)" : "=s"(dg_t1), "=s"(dg_c1) :: "memory");
    const u64 until = dg_t0 + 4000; while (__builtin_amdgcn_s_memrealtime() < until) __builtin_amdgcn_s_sleep(1); }
}

extern "C" void kernel_launch(void* const* d_in, const int* in_sizes, int n_in, void* d_out, int out_size, void* d_ws, size_t ws_size,
                              hipStream_t stream) {
  gqa_fused<<<256, 512, 0, stream>>>(d_in[0], d_in[1], d_in[2], d_in[3], d_in[4], d_out, (char*)d_ws
  );
}
```

```cpp
#include <hip/hip_runtime.h>
#include <hip/hip_bf16.h>

typedef unsigned long long u64;
typedef unsigned u32;
typedef u32 v4u __attribute__((ext_vector_type(4)));
typedef u32 v2u __attribute__((ext_vector_type(2)));
typedef short s16x4 __attribute__((ext_vector_type(4)));
typedef short bf16x8 __attribute__((ext_vector_type(8)));
typedef float f32x16 __attribute__((ext_vector_type(16)));
typedef float f32x4 __attribute__((ext_vector_type(4)));
typedef float f32x2 __attribute__((ext_vector_type(2)));
typedef __bf16 b16x2 __attribute__((ext_vector_type(2)));
#define LAS __attribute__((address_space(3)))
#define GAS __attribute__((address_space(1)))
#define DI __device__ __forceinline__
typedef __amdgpu_buffer_rsrc_t rsrc_t;

constexpr u32 PG = 8192;
constexpr u32 SLAB = 32768;
constexpr u32 IN9 = 16384, POUT = 24576, CMAIN = 28672, C9 = 29696, CP = 30208, EPOCH = 31744;
constexpr u32 WS_BYTES = 256 * SLAB;
constexpr u32 OOB = 0x80000000u;
constexpr int SPIN_CAP = 1 << 16;
#define CODE_BYTES 17920
#define PACE 6
#define PSLEEP 1
#define KPOS 0
#define SH0 1
#define SH1 2
#define SH2 3
#define SH3 4
#define HPRIO 2
#define PPOS 3
#define GSLEEP 20

#define STAMP(i)
#define SB() __builtin_amdgcn_sched_barrier(0)
#define HT(k, N) asm volatile("s_waitcnt vmcnt(%0) lgkmcnt(0)\n\ts_barrier" :: "i"(N) : "memory")
#define CT(k) do { SB(); asm volatile("s_waitcnt lgkmcnt(0)\n\ts_barrier" ::: "memory"); SB(); } while (0)
#define CB(k) do { SB(); asm volatile("s_barrier" ::: "memory"); SB(); } while (0)
#define HV(k, N) asm volatile("s_waitcnt vmcnt(%0)\n\ts_barrier" :: "i"(N) : "memory")
#define WV(N) asm volatile("s_waitcnt vmcnt(%0)" :: "i"(N) : "memory")
#define PIN(v) asm volatile("" : "+v"(v))

template <bool NT> DI void dma(u32 lds, u32 voff, rsrc_t rs, u32 soff) {
  if constexpr (NT) asm volatile("s_mov_b32 m0, %0\n\ts_nop 0\n\tbuffer_load_dwordx4 %1, %2, %3 offen nt lds" :: "s"(lds), "v"(voff), "s"(rs), "s"(soff) : "memory");
  else              asm volatile("s_mov_b32 m0, %0\n\ts_nop 0\n\tbuffer_load_dwordx4 %1, %2, %3 offen lds" :: "s"(lds), "v"(voff), "s"(rs), "s"(soff) : "memory");
}
DI void st16(v4u v, u32 voff, rsrc_t rs, u32 soff) {
  asm volatile("buffer_store_dwordx4 %0, %1, %2, %3 offen sc1\n\ts_nop 1" :: "v"(v), "v"(voff), "s"(rs), "s"(soff) : "memory");
}
DI void st8(v2u v, u32 voff, rsrc_t rs, u32 soff) {
  asm volatile("buffer_store_dwordx2 %0, %1, %2, %3 offen sc1" :: "v"(v), "v"(voff), "s"(rs), "s"(soff) : "memory");
}
DI s16x4 tr4(u32 a) { return __builtin_amdgcn_ds_read_tr16_b64_v4i16((LAS s16x4*)(size_t)a); }
DI bf16x8 trd(u32 a0, u32 a1) { return __builtin_shufflevector(tr4(a0), tr4(a1), 0, 1, 2, 3, 4, 5, 6, 7); }
DI bf16x8 rd16(u32 a) { return *(LAS bf16x8*)(size_t)a; }
DI u32 pk(float a, float b) { f32x2 v = {a, b}; return __builtin_bit_cast(u32, __builtin_convertvector(v, b16x2)); }
template <int S> DI v4u pack8(const f32x16& c) {
  v4u r = {pk(c[8 * S], c[8 * S + 1]), pk(c[8 * S + 2], c[8 * S + 3]), pk(c[8 * S + 4], c[8 * S + 5]), pk(c[8 * S + 6], c[8 * S + 7])};
  PIN(r);
  return r;
}
DI f32x16 MF32(bf16x8 a, bf16x8 b, f32x16 c) { SB(); f32x16 d = __builtin_amdgcn_mfma_f32_32x32x16_bf16(a, b, c, 0, 0, 0); PIN(d); SB(); return d; }
DI f32x4 MS16(bf16x8 a, bf16x8 b, f32x4 c) { SB(); f32x4 d = __builtin_amdgcn_mfma_f32_16x16x32_bf16(a, b, c, 0, 0, 0); PIN(d); SB(); return d; }
#define MF16(a, b, c) __builtin_amdgcn_mfma_f32_16x16x32_bf16((a), (b), (c), 0, 0, 0)
#define BC(v) __builtin_bit_cast(bf16x8, (v))
template <int C> DI float dppf(float v) { return __builtin_bit_cast(float, __builtin_amdgcn_mov_dpp(__builtin_bit_cast(int, v), C, 0xF, 0xF, true)); }
template <int C> DI u32 dppu(u32 v) { return (u32)__builtin_amdgcn_mov_dpp((int)v, C, 0xF, 0xF, true); }
DI u32 xor3(u32 a, u32 b, u32 c) { return __builtin_amdgcn_bitop3_b32(a, b, c, 0x96); }
DI v2u fold(v4u d) { return v2u{(d.x + d.y + d.z) + d.w, xor3(d.x, d.y, d.z) ^ d.w}; }
DI v2u fold(v4u d, v2u a) { return v2u{(d.x + d.y + a.x) + d.z + d.w, xor3(xor3(d.x, d.y, a.y), d.z, d.w)}; }
DI v2u row8(v2u v) { return v2u{v.x + dppu<0x128>(v.x), v.y ^ dppu<0x128>(v.y)}; }
DI v2u half8(v2u v) {
  v.x += dppu<0x141>(v.x); v.y ^= dppu<0x141>(v.y); v.x += dppu<0x4E>(v.x); v.y ^= dppu<0x4E>(v.y);
  v.x += dppu<0xB1>(v.x); v.y ^= dppu<0xB1>(v.y);
  return v;
}

__global__ void __launch_bounds__(512, 2)
gqa_fused(const void* __restrict__ x, const void* __restrict__ kc, const void* __restrict__ vc, const void* __restrict__ wq,
          const void* __restrict__ wo, void* __restrict__ out, char* __restrict__ ws
) {
  __shared__ __attribute__((aligned(1024))) char smem[20 * PG];
  u64 pc0;
  asm volatile("s_getpc_b64 %0" : "=s"(pc0));
  const u32 L = (u32)(size_t)(LAS char*)smem;
  const u32 tid = threadIdx.x, lane = tid & 63;
  const u32 w = __builtin_amdgcn_readfirstlane(tid >> 6);
  const u32 b = blockIdx.x, hq = b & 7, bp = b >> 5, i = (b >> 3) & 3, h = 4 * hq + i;
  STAMP(0);
  const rsrc_t rw = __builtin_amdgcn_make_buffer_rsrc((void*)ws, 0, WS_BYTES, 0x00020000);
  const rsrc_t ry = __builtin_amdgcn_make_buffer_rsrc(out, 0, 512 * 1152, 0x00020000);
  const u32 me = hq * 4 + i, row = lane >> 4, col = lane & 15;
  const u32 hh = lane >> 5, l31 = lane & 31;
  const u32 ct = w & 3, bb = w >> 2, q4 = col >> 2, p4 = lane & 3;
  f32x16 zero;
#pragma unroll
  for (int n = 0; n < 16; ++n) zero[n] = 0.f;
  u32 tag, vy, vg, vgc, v16, vcm, ap, aw[2][2], av[8]; v4u sel;
  bf16x8 vf[8], bw[8]; v4u uu[4][2]; f32x16 u;
  auto setup2 = [&]() {
    const u32 one = (col & 1) ? 0x3F800000u : 0x00003F80u;
    sel = v4u{(col >> 1) == 0 ? one : 0u, (col >> 1) == 1 ? one : 0u, (col >> 1) == 2 ? one : 0u, (col >> 1) == 3 ? one : 0u};
    const u32 yrow = 64 * bp + 32 * (hq >> 2) + 16 * ((hq >> 1) & 1) + (col & 3) + 8 * ((col >> 2) & 1);
    vy = col < 8 ? yrow * 1152 + 8 * row : OOB;
    vg = row * 2048 + w * 256 + col * 16 + b * SLAB; vgc = row * 128 + w * 8 + b * SLAB + CMAIN;
    v16 = lane * 16; vcm = (col & 7) == 0 ? (col >> 3) * (2 * SLAB) + row * 8 : OOB;
    ap = L + 2 * PG + bb * 2048 + lane * 16;
    PIN(sel); PIN(vy); PIN(vg); PIN(vgc); PIN(v16); PIN(vcm); PIN(ap);
#pragma unroll
    for (int t = 0; t < 2; ++t) {
      aw[0][t] = L + 256 * (8 * hh + 4 * t + q4) + 16 * ((4 * ct + 2 * (row & 1) + (p4 >> 1)) ^ ((q4 << 2) | (2 * hh + t))) + 8 * (p4 & 1);
      aw[1][t] = aw[0][t] + 8 * PG;
      PIN(aw[0][t]); PIN(aw[1][t]);
    }
    const u32 arow = 256 * l31 + 16 * (hh ^ (((l31 & 3) << 2) | ((l31 >> 2) & 3)));
#pragma unroll
    for (int n = 0; n < 8; ++n) { av[n] = L + (16 + bb) * PG + (arow ^ (128 * (bp & 1)) ^ (32 * n)); PIN(av[n]); }
  };
  auto rV = [&](int slot, int s) { vf[s] = rd16(av[s] + 2 * slot * PG); };
  auto rW = [&](int s, int pg) { const u32 o = (pg & 7) * PG + (s & 1) * 4096; bw[s] = trd(aw[pg >> 3][0] + o, aw[pg >> 3][1] + o); };
  auto headW = [&](int hs, int p0, int p1, int p2, int p3, auto f) {
    const int pg[4] = {p0, p1, p2, p3};
    const f32x16 o = u;
    rW(0, p0); SB(); rW(1, p0); SB(); rW(2, p1); SB(); rW(3, p1); SB();
    uu[hs - 1][0] = pack8<0>(o); uu[hs - 1][1] = pack8<1>(o);
#pragma unroll
    for (int s = 0; s < 8; ++s) {
      u = MF32(vf[s], bw[s], s ? u : zero);
      if (s < 4) rW(s + 4, pg[2 + (s >> 1)]);
      if (hs == 1) rV(1, s);
      f(s);
    }
  };
  auto lagW = [&](int hs, int p0, int p1, int p2, int p3, auto f) {
    const int pg[4] = {p0, p1, p2, p3};
#pragma unroll
    for (int s = 4; s < 8; ++s) { u = MF32(vf[s], bw[s], u); rW(s - 4, pg[(s - 4) >> 1]); if (hs == 2) rV(1, s); f(s - 4); }
    const f32x16 o = u;
#pragma unroll
    for (int s = 0; s < 4; ++s) {
      u = MF32(vf[s], bw[s], s ? u : zero);
      rW(s + 4, pg[2 + (s >> 1)]);
      if (hs == 1) rV(1, s);
      if (s == 2) uu[hs - 1][0] = pack8<0>(o);
      if (s == 3) uu[hs - 1][1] = pack8<1>(o);
      f(s + 4);
    }
  };
  auto nop = [](int) {};
  asm volatile("" :: "s"(x), "s"(kc), "s"(vc), "s"(wq), "s"(wo), "s"(out), "s"(ws));

  if (__builtin_expect(w >= 4, 1)) {
    const u32 g = w - 4;
    const rsrc_t rx = __builtin_amdgcn_make_buffer_rsrc((void*)x, 0, 512 * 1152, 0x00020000);
    const rsrc_t rk = __builtin_amdgcn_make_buffer_rsrc((void*)kc, 0, 256 * 8192, 0x00020000);
    const rsrc_t rv = __builtin_amdgcn_make_buffer_rsrc((void*)vc, 0, 256 * 8192, 0x00020000);
    const rsrc_t rq = __builtin_amdgcn_make_buffer_rsrc((void*)wq, 0, 576 * 8192, 0x00020000);
    const rsrc_t ro = __builtin_amdgcn_make_buffer_rsrc((void*)wo, 0, 4096 * 1152, 0x00020000);
    const u32 xr = 8 * g + (lane >> 3);
    const u32 vx = xr * 1152 + 16 * ((lane & 7) ^ ((xr >> 1) & 7));
    const u32 pr = 4 * g + row;
    const u32 pc = col ^ ((row << 2) | g);
    const u32 vq = pr * 8192 + 16 * pc;
    const u32 Lg = L + 1024 * g;
    const u32 r0 = 3 * ((bp + i) % 3);
    const u32 sx = bp * (64 * 1152), sq = 256 * h;
    auto step = [&](int s) {
      u32 ks = r0 + s; ks = ks >= 9 ? ks - 9 : ks;
      const u32 o = sx + ks * 128, o2 = sq + ks * (64 * 8192);
      dma<false>(Lg + (8 + (s & 3)) * PG, vx, rx, o); dma<false>(Lg + (8 + (s & 3)) * PG + 4096, vx, rx, o + 32 * 1152);
#pragma unroll
      for (int n = 0; n < 4; ++n) dma<true>(Lg + 2 * (s & 3) * PG + n * 4096, vq, rq, o2 + n * (16 * 8192));
    };
    step(0); step(1);
    {
      const rsrc_t rc = __builtin_amdgcn_make_buffer_rsrc((void*)(pc0 & ~255ull), 0, CODE_BYTES, 0x00020000);
      asm volatile("s_buffer_load_dword s100, %0, %1\n\ts_buffer_load_dword s101, %2, %3\n\ts_buffer_load_dword s101, %2, %3 offset:64\n\t"
                   "s_buffer_load_dword s101, %2, %3 offset:128\n\ts_buffer_load_dword s101, %4, %5"
                   :: "s"(rw), "s"(bp * (32 * SLAB) + EPOCH), "s"(rc), "s"(((b >> 3) * 4 + g) * 192), "s"(ry), "s"(bp * (64 * 1152)) : "s100", "s101", "memory");
    }
    u32 vkv = pr * 256 + 16 * pc, vo = pr * 1152 + 16 * pc, vo9 = (pr + 64 * (pc >> 3)) * 1152 + 16 * (pc & 7);
    u32 pl = L + 2 * PG + g * 4096 + row * 512 + col * 16;
    u32 vcl = row * 8;
    PIN(vkv); PIN(vo); PIN(vo9); PIN(pl); PIN(vcl);
    u32 bs = b;
    asm volatile("" : "+s"(bs));
    setup2();
    auto ldk = [&](u32 n) {
      const u32 o = ((2 * bp + n) * 16 + (h >> 1)) * 8192;
      dma<true>(Lg + (12 + n) * PG, vkv, rk, o); dma<true>(Lg + (12 + n) * PG + 4096, vkv, rk, o + 4096);
    };
    auto ldv = [&](u32 n) {
      const u32 o = ((2 * bp + (n & 1)) * 16 + 2 * hq + ((n >> 1) ^ (bp >> 2))) * 8192;
      dma<true>(Lg + (16 + n) * PG, vkv, rv, o); dma<true>(Lg + (16 + n) * PG + 4096, vkv, rv, o + 4096);
    };
    auto ldw1 = [&](int n, int pg, int k) {
      dma<true>(Lg + pg * PG + k * 4096, vo, ro, ((bs & 7) * 512 + 32 * (n ^ ((bs >> 5) << 1)) + 16 * k) * 1152 + 256 * ((bs >> 3) & 3));
    };
    auto ldw = [&](int n, int pg) { ldw1(n, pg, 0); ldw1(n, pg, 1); };
    auto ldn = [&](int n, int pg) {
      const u32 o = (h * 128 + 32 * n) * 1152 + 1024;
      dma<true>(Lg + pg * PG, vo9, ro, o); dma<true>(Lg + pg * PG + 4096, vo9, ro, o + 16 * 1152);
    };
    auto extra = [&](int t) {
      if (t == KPOS) { ldk(i & 1); ldk((i & 1) ^ 1); }
      const u32 grp = bp & 3;
      if ((t == SH0 && grp == 0) || (t == SH1 && grp == 1) || (t == SH2 && grp == 2) || (t == SH3 && grp == 3)) {
        ldv(i); ldw(0, 14); ldw(1, 15);
        dma<true>(Lg + (16 + (i ^ 1)) * PG, vo9, ro, (h * 128 + 16 * (bp & 3)) * 1152 + 1024);
      }
    };
    step(2);
    HV(0, 12); step(3); extra(0);
    HV(1, 12); step(4); extra(1);
    HV(2, 12); step(5); extra(2);
    HV(3, 12); step(6); extra(3);
    HV(4, 12); step(7); extra(4);
    HV(5, 12); step(8);
    HV(6, 12); ldv(i ^ 1); ldv(i ^ 2);
    HV(7, 10); ldv(i ^ 3);
    asm volatile("s_waitcnt lgkmcnt(0)\n\ts_mov_b32 %0, s100" : "=s"(tag) :: "memory");
    tag += 1;
    HV(8, 8);  ldw(2, 9);
    HV(9, 8);  WV(PACE); ldw(3, 10); WV(PACE); ldn(0, 2); WV(PACE); ldn(1, 3);
    HV(10, 8);
    rV(0, 0); rW(0, 14); SB(); rV(0, 1); rW(1, 14); SB(); rV(0, 2); rW(2, 15); SB(); rV(0, 3); rW(3, 15);
#pragma unroll
    for (int s = 0; s < 4; ++s) { u = MF32(vf[s], bw[s], s ? u : zero); rV(0, s + 4); }
    WV(PACE); ldw(4, 4); WV(PACE); ldw(5, 5); WV(PACE); ldw(6, 6); WV(PACE); ldw(7, 7);
    HV(11, 8);
    rW(4, 9); SB(); rW(5, 9); SB(); rW(6, 10); SB(); rW(7, 10); SB();
    ldw(8, 11);
#pragma unroll
    for (int s = 4; s < 8; ++s) u = MF32(vf[s], bw[s], u);
    WV(PACE); ldw(9, 0); WV(PACE); ldw(10, 1); WV(PACE); ldw(11, 8);
    __builtin_amdgcn_s_setprio(HPRIO);
    HV(12, 8);
    headW(1, 4, 5, 6, 7, [&](int s) { ldw1(12 + (s >> 1), 12 + (s >> 1), s & 1); });
    HV(13, 8);
    v4u p0, p1, p2, p3; v2u c0, c1, c2, c3;
    const u32 spl = ((bp << 5) | ((g ^ (bp >> 1)) << 3) | hq) * SLAB;
#define LOAD_P "buffer_load_dwordx4 %0, %8, %10, %11 offen sc1\n\tbuffer_load_dwordx4 %1, %8, %10, %11 offen offset:1024 sc1\n\t" \
               "buffer_load_dwordx4 %2, %8, %10, %11 offen offset:2048 sc1\n\tbuffer_load_dwordx4 %3, %8, %10, %11 offen offset:3072 sc1\n\t" \
               "buffer_load_dwordx2 %4, %9, %10, %12 offen sc1\n\tbuffer_load_dwordx2 %5, %9, %10, %12 offen offset:32 sc1\n\t" \
               "buffer_load_dwordx2 %6, %9, %10, %12 offen offset:64 sc1\n\tbuffer_load_dwordx2 %7, %9, %10, %12 offen offset:96 sc1"
#define P_ARGS "=&v"(p0), "=&v"(p1), "=&v"(p2), "=&v"(p3), "=&v"(c0), "=&v"(c1), "=&v"(c2), "=&v"(c3) : "v"(v16), "v"(vcl), "s"(rw), "s"(spl + POUT), "s"(spl + CP) : "memory"
    headW(2, 11, 0, 1, 8, [&](int s) { if (s == PPOS) asm volatile(LOAD_P : P_ARGS); });
    HV(14, 8);
    headW(3, 12, 13, 14, 15, nop);
    asm volatile("s_waitcnt vmcnt(0)" : "+v"(p0), "+v"(p1), "+v"(p2), "+v"(p3), "+v"(c0), "+v"(c1), "+v"(c2), "+v"(c3) :: "memory");
    STAMP(62);
    _Pragma("nounroll") for (int sp = 0; sp < SPIN_CAP; ++sp) {
      *(LAS v4u*)(size_t)pl = p0; *(LAS v4u*)(size_t)(pl + 256) = p1; *(LAS v4u*)(size_t)(pl + 2048) = p2; *(LAS v4u*)(size_t)(pl + 2304) = p3;
      const v2u f0 = fold(p1, fold(p0)), f1 = fold(p3, fold(p2));
      const v2u f = half8(row8(v2u{f0.x + f1.x, f0.y ^ f1.y}));
      if (__all(f.x + 4 * tag == (c0.x + c1.x) + (c2.x + c3.x) && f.y == (xor3(c0.y, c1.y, c2.y) ^ c3.y))) break;
      asm volatile("s_waitcnt lgkmcnt(0)\n\t" LOAD_P "\n\ts_waitcnt vmcnt(0)" : P_ARGS);
    }
    HT(15, 0);
  } else {
    setup2();
    const u32 mt = w >> 1;
    u32 aq[8][2], ax[2], ak[8], a9[4][2];
    const u32 xrow = 16 * w + col, key = 16 * (col >> 3) + (col & 7);
    ax[0] = L + 8 * PG + 128 * xrow + 16 * (row ^ ((xrow >> 1) & 7)); ax[1] = ax[0] ^ 64;
    PIN(ax[0]); PIN(ax[1]);
#pragma unroll
    for (int n = 0; n < 8; ++n) {
#pragma unroll
      for (int t = 0; t < 2; ++t) {
        aq[n][t] = L + 256 * (8 * row + 4 * t + q4) + 16 * ((2 * n + (p4 >> 1)) ^ ((q4 << 2) | ((2 * row + t) & 3))) + 8 * (p4 & 1);
        PIN(aq[n][t]);
      }
      ak[n] = L + (12 + mt) * PG + 256 * key + 16 * ((2 * n + (row >> 1)) ^ (((key & 3) << 2) | ((key >> 2) & 3))) + 8 * (row & 1);
      a9[n >> 1][n & 1] = L + (16 + mt + 2 * ((i >> 1) ^ (bp >> 2))) * PG + 256 * (16 * (row >> 1) + 4 * (row & 1) + 8 * (n & 1) + q4) +
                          16 * ((4 * (n >> 1) + p4) ^ ((q4 << 2) | ((row & 1) + 2 * (n & 1))));
      PIN(ak[n]); PIN(a9[n >> 1][n & 1]);
    }
    u32 v9 = (row << 3) * SLAB + col * 16, vc9 = (col & 7) == 0 ? (row << 3) * SLAB + (col >> 3) * SLAB : OOB, vcs = col == 0 ? row * 8 : OOB;
    PIN(v9); PIN(vc9); PIN(vcs);
    const u32 sp0 = b * SLAB + POUT + w * 1024, sp1 = b * SLAB + CP + w * 32;
    const u32 s9 = ((bp << 5) | (2 * w)) * SLAB;
    const u32 s9a = s9 + IN9 + me * 256, s9b = s9a + SLAB, s9c = s9 + C9 + me * 8;
    asm volatile("" :: "s"(sp0), "s"(sp1), "s"(s9a), "s"(s9b), "s"(s9c));

    const f32x4 z4 = {0.f, 0.f, 0.f, 0.f};
    f32x4 qa[8];
#pragma unroll
    for (int n = 0; n < 8; ++n) qa[n] = z4;
    bf16x8 A[16], B[2], kf[2][4];
    auto rA = [&](int sl, int n) { const u32 o = (2 * sl + (n >> 3)) * PG; A[n] = trd(aq[n & 7][0] + o, aq[n & 7][1] + o); };
    auto rB = [&](int sl, int k) { B[k] = rd16(ax[k] + sl * PG); };
    auto rK = [&](int T, int ks) {
      const s16x4 lo = *(LAS s16x4*)(size_t)(ak[2 * ks + T] + T * 2048), hi = *(LAS s16x4*)(size_t)(ak[2 * ks + 1 - T] + T * 2048);
      kf[T][ks] = __builtin_shufflevector(lo, hi, 0, 1, 2, 3, 4, 5, 6, 7);
    };
    bf16x8 Bc;
    auto head = [&](int sl, bool carry) {
      rB(sl, 0);
#pragma unroll
      for (int n = 0; n < 6; ++n) { rA(sl, n); if (carry) qa[n + 2] = MS16(A[n + 10], Bc, qa[n + 2]); else SB(); }
      rB(sl, 1);
    };
    auto p1 = [&](int t) {
      head(t & 3, t > 0);
#pragma unroll
      for (int n = 0; n < 10; ++n) { qa[n & 7] = MS16(A[n], B[n >> 3], qa[n & 7]); rA(t & 3, n + 6); }
      Bc = B[1];
      if (t == KPOS + 4) { rK(0, 0); rK(0, 1); rK(0, 2); rK(0, 3); }
      if (t == KPOS + 5) { rK(1, 0); rK(1, 1); rK(1, 2); rK(1, 3); }
    };
    CT(0); p1(0); CT(1); p1(1); CT(2); p1(2); CT(3); p1(3);
    asm volatile("s_buffer_load_dword %0, %1, %2\n\ts_waitcnt lgkmcnt(0)" : "=s"(tag) : "s"(rw), "s"(bp * (32 * SLAB) + EPOCH) : "memory");
    tag += 1;
    CT(4); p1(4); CT(5); p1(5); CB(6); p1(6); CB(7); p1(7);
    CB(8);
    head(0, true);
#pragma unroll
    for (int n = 0; n < 8; ++n) { qa[n] = MS16(A[n], B[0], qa[n]); if (n < 2) rA(0, n + 6); }
    CB(9);
    f32x4 sa = z4, sb = z4;
    {
      v4u q[4];
      auto pq = [&](int ks) { q[ks] = v4u{pk(qa[2 * ks][0], qa[2 * ks][1]), pk(qa[2 * ks][2], qa[2 * ks][3]), pk(qa[2 * ks + 1][0], qa[2 * ks + 1][1]), pk(qa[2 * ks + 1][2], qa[2 * ks + 1][3])}; PIN(q[ks]); };
#pragma unroll
      for (int n = 8; n < 14; ++n) { rA(0, n); SB(); }
      qa[0] = MS16(A[8], B[1], qa[0]); rA(0, 14);
      qa[1] = MS16(A[9], B[1], qa[1]); rA(0, 15);
      qa[2] = MS16(A[10], B[1], qa[2]);
      qa[3] = MS16(A[11], B[1], qa[3]);
      qa[4] = MS16(A[12], B[1], qa[4]);
      qa[5] = MS16(A[13], B[1], qa[5]);
      pq(0);
      qa[6] = MS16(A[14], B[1], qa[6]);
      qa[7] = MS16(A[15], B[1], qa[7]);
      pq(1);
      sa = MS16(kf[0][0], BC(q[0]), sa); sb = MS16(kf[1][0], BC(q[0]), sb);
      pq(2);
      sa = MS16(kf[0][1], BC(q[1]), sa); sb = MS16(kf[1][1], BC(q[1]), sb);
      pq(3);
      sa = MS16(kf[0][2], BC(q[2]), sa); sb = MS16(kf[1][2], BC(q[2]), sb);
      sa = MS16(kf[0][3], BC(q[3]), sa); sb = MS16(kf[1][3], BC(q[3]), sb);
    }
    CB(10);
    {
      bf16x8 vt[8], wt[16]; f32x4 ot[8], y[4]; v4u of[4];
      auto rT = [&](int n) { vt[n] = trd(a9[n >> 1][0] + 8 * (n & 1), a9[n >> 1][1] + 8 * (n & 1)); };
      auto rN = [&](int n) { const int ks = n >> 2, c = 4 * (ks >> 1) + (n & 3); wt[n] = trd(aq[c][0] + (2 + (ks & 1)) * PG, aq[c][1] + (2 + (ks & 1)) * PG); };
      rT(0); rT(1); rT(2); rT(3); SB();
      float mx, m2;
      asm volatile("s_nop 1\n\tv_max3_f32 %0, %2, %3, %4\n\tv_max3_f32 %1, %5, %6, %7\n\tv_max3_f32 %0, %0, %8, %9\n\tv_max_f32 %0, %0, %1\n\t"
                   "v_mov_b32 %1, %0\n\ts_nop 1\n\tv_permlane16_swap_b32 %0, %1\n\tv_max_f32 %0, %0, %1\n\t"
                   "v_mov_b32 %1, %0\n\ts_nop 1\n\tv_permlane32_swap_b32 %0, %1\n\tv_max_f32 %0, %0, %1"
                   : "=&v"(mx), "=&v"(m2) : "v"(sa[0]), "v"(sa[1]), "v"(sa[2]), "v"(sa[3]), "v"(sb[0]), "v"(sb[1]), "v"(sb[2]), "v"(sb[3]));
      const float c = 0.08838834764831845f * 1.4426950408889634f, mc = -mx * c;
      float e[8], z2;
#pragma unroll
      for (int n = 0; n < 4; ++n) { e[n] = __builtin_amdgcn_exp2f(__builtin_fmaf(sa[n], c, mc)); e[4 + n] = __builtin_amdgcn_exp2f(__builtin_fmaf(sb[n], c, mc)); }
      float z = ((e[0] + e[2]) + (e[4] + e[6])) + ((e[1] + e[3]) + (e[5] + e[7]));
      asm volatile("v_mov_b32 %1, %0\n\ts_nop 1\n\tv_permlane16_swap_b32 %0, %1\n\tv_add_f32 %0, %0, %1\n\t"
                   "v_mov_b32 %1, %0\n\ts_nop 1\n\tv_permlane32_swap_b32 %0, %1\n\tv_add_f32 %0, %0, %1" : "+v"(z), "=&v"(z2));
      const float r = __builtin_amdgcn_rcpf(z);
      const v4u pv = {pk(e[0] * r, e[1] * r), pk(e[2] * r, e[3] * r), pk(e[4] * r, e[5] * r), pk(e[6] * r, e[7] * r)};
      st16(pv, v16, rw, sp0);
      rT(4); rT(5); rT(6); rT(7);
      v2u ck;
#pragma unroll
      for (int n = 0; n < 8; ++n) {
        ot[n] = MS16(vt[n], BC(pv), z4);
        if (n == 0) { ck = row8(fold(pv)); PIN(ck); } else if (n == 1) { ck = half8(ck); ck.x += tag; st8(ck, vcs, rw, sp1); }
        else if (n >= 4) { rV(0, n - 4); rW(n - 4, 14 + ((n - 4) >> 1)); }
      }
      STAMP(61);
      CB(11);
#pragma unroll
      for (int s = 0; s < 4; ++s) {
        rN(2 * s); rN(2 * s + 1);
        of[s] = v4u{pk(ot[2 * s][0], ot[2 * s][1]), pk(ot[2 * s][2], ot[2 * s][3]), pk(ot[2 * s + 1][0], ot[2 * s + 1][1]), pk(ot[2 * s + 1][2], ot[2 * s + 1][3])};
        PIN(of[s]);
        u = MF32(vf[s], bw[s], s ? u : zero);
      }
#pragma unroll
      for (int n = 0; n < 16; ++n) {
        y[n & 3] = MS16(wt[n], BC(of[n >> 2]), n < 4 ? z4 : y[n & 3]);
        if (n < 8) rN(n + 8); else if (n & 1) rW(4 + ((n - 8) >> 1), 9 + ((n - 8) >> 2)); else rV(0, 4 + ((n - 8) >> 1));
      }
      v4u e0, e1; v2u g0, g1;
      CB(12);
      lagW(1, 4, 5, 6, 7, [&](int s) {
        if (s == 0) { e0 = v4u{pk(y[0][0], y[0][1]), pk(y[0][2], y[0][3]), pk(y[1][0], y[1][1]), pk(y[1][2], y[1][3])}; st16(e0, v9, rw, s9a); }
        else if (s == 1) { e1 = v4u{pk(y[2][0], y[2][1]), pk(y[2][2], y[2][3]), pk(y[3][0], y[3][1]), pk(y[3][2], y[3][3])}; st16(e1, v9, rw, s9b); }
        else if (s == 2) { g0 = fold(e0); PIN(g0); } else if (s == 3) { g1 = fold(e1); PIN(g1); }
        else if (s == 4) { g0 = row8(g0); g1 = row8(g1); g0 = col < 8 ? g0 : g1; PIN(g0); }
        else if (s == 5) { g0 = half8(g0); g0.x += tag; st8(g0, vc9, rw, s9c); } });
    }
    CB(13); lagW(2, 11, 0, 1, 8, nop);
    CB(14); lagW(3, 12, 13, 14, 15, nop);
#pragma unroll
    for (int s = 4; s < 8; ++s) u = MF32(vf[s], bw[s], u);
    CB(15);
  }
  {
    const u32 so = ((bp << 5) | (i << 3) | (bb * 4 + (ct >> 1))) * SLAB;
    const u32 sd0 = __builtin_amdgcn_readfirstlane(so + hq * 2048 + (ct & 1) * 1024), sd1 = sd0 + 2 * SLAB;
    const u32 sc0 = __builtin_amdgcn_readfirstlane(so + CMAIN + hq * 128 + (ct & 1) * 32);
    bf16x8 pf[8];
#pragma unroll
    for (int n = 0; n < 8; ++n) { pf[n] = rd16(ap + (n >> 1) * 4096 + (n & 1) * 1024); SB(); }
    uu[3][0] = pack8<0>(u); uu[3][1] = pack8<1>(u);
    f32x16 o = zero;
#pragma unroll
    for (int n = 0; n < 8; ++n) o = MF32(pf[n], BC(uu[n >> 1][n & 1]), o);
    const v4u d0 = pack8<0>(o); st16(d0, v16, rw, sd0);
    const v4u d1 = pack8<1>(o); st16(d1, v16, rw, sd1);
    const v2u f0 = row8(fold(d0)), f1 = row8(fold(d1));
    v2u f = half8(col < 8 ? f0 : f1); f.x += tag; st8(f, vcm, rw, sc0);
  }
  STAMP(58);
  if (w == 7) {
    const u32 vd = v16 + b * SLAB + IN9, vc = row * 8 + b * SLAB + C9;
    v4u d[8]; v2u c[8];
    _Pragma("nounroll") for (int sp = 0; sp < SPIN_CAP; ++sp) {
      asm volatile("buffer_load_dwordx4 %0, %16, %18, 0 offen sc1\n\tbuffer_load_dwordx4 %1, %16, %18, 0 offen offset:1024 sc1\n\t"
                   "buffer_load_dwordx4 %2, %16, %18, 0 offen offset:2048 sc1\n\tbuffer_load_dwordx4 %3, %16, %18, 0 offen offset:3072 sc1\n\t"
                   "buffer_load_dwordx4 %4, %16, %18, %19 offen sc1\n\tbuffer_load_dwordx4 %5, %16, %18, %19 offen offset:1024 sc1\n\t"
                   "buffer_load_dwordx4 %6, %16, %18, %19 offen offset:2048 sc1\n\tbuffer_load_dwordx4 %7, %16, %18, %19 offen offset:3072 sc1\n\t"
                   "buffer_load_dwordx2 %8, %17, %18, 0 offen sc1\n\tbuffer_load_dwordx2 %9, %17, %18, 0 offen offset:32 sc1\n\t"
                   "buffer_load_dwordx2 %10, %17, %18, 0 offen offset:64 sc1\n\tbuffer_load_dwordx2 %11, %17, %18, 0 offen offset:96 sc1\n\t"
                   "buffer_load_dwordx2 %12, %17, %18, 0 offen offset:128 sc1\n\tbuffer_load_dwordx2 %13, %17, %18, 0 offen offset:160 sc1\n\t"
                   "buffer_load_dwordx2 %14, %17, %18, 0 offen offset:192 sc1\n\tbuffer_load_dwordx2 %15, %17, %18, 0 offen offset:224 sc1\n\t"
                   "s_waitcnt vmcnt(0)"
                   : "=&v"(d[0]), "=&v"(d[1]), "=&v"(d[2]), "=&v"(d[3]), "=&v"(d[4]), "=&v"(d[5]), "=&v"(d[6]), "=&v"(d[7]),
                     "=&v"(c[0]), "=&v"(c[1]), "=&v"(c[2]), "=&v"(c[3]), "=&v"(c[4]), "=&v"(c[5]), "=&v"(c[6]), "=&v"(c[7])
                   : "v"(vd), "v"(vc), "s"(rw), "s"(4096) : "memory");
      const v2u f0 = fold(d[3], fold(d[2], fold(d[1], fold(d[0])))), f1 = fold(d[7], fold(d[6], fold(d[5], fold(d[4]))));
      const v2u f = half8(row8(v2u{f0.x + f1.x, f0.y ^ f1.y}));
      u32 cs = 0, cx = 0;
#pragma unroll
      for (int n = 0; n < 8; ++n) { cs += c[n].x; cx ^= c[n].y; }
      if (__all(f.x + 8 * tag == cs && f.y == cx)) break;
      __builtin_amdgcn_s_sleep(2);
    }
    f32x4 y = {0.f, 0.f, 0.f, 0.f};
#pragma unroll
    for (int n = 0; n < 8; ++n) y = MF16(BC(sel), BC(d[n]), y);
    __builtin_amdgcn_raw_buffer_store_b64(v2u{pk(y[0], y[1]), pk(y[2], y[3])}, ry,
                                          row < 2 ? (64 * bp + 16 * (hq >> 1) + col) * 1152 + row * 32 + (512 + 32 * (hq & 1) + 4 * i) * 2 : OOB, 0, 0);
    if (me == 0) __builtin_amdgcn_raw_buffer_store_b32(tag, rw, lane == 0 ? bp * (32 * SLAB) + EPOCH : OOB, 0, 16);
  } else {
    asm volatile("s_waitcnt vmcnt(0)" ::: "memory");
  }
  STAMP(59);
  {
    v4u ga, gb; v2u ca, cb;
    _Pragma("nounroll") for (int sp = 0; sp < SPIN_CAP; ++sp) {
      asm volatile("buffer_load_dwordx4 %0, %4, %7, 0 offen sc1\n\tbuffer_load_dwordx4 %1, %4, %7, %5 offen sc1\n\t"
                   "buffer_load_dwordx2 %2, %6, %7, 0 offen sc1\n\tbuffer_load_dwordx2 %3, %6, %7, 0 offen offset:512 sc1\n\ts_waitcnt vmcnt(0)"
                   : "=&v"(ga), "=&v"(gb), "=&v"(ca), "=&v"(cb) : "v"(vg), "s"(8192), "v"(vgc), "s"(rw) : "memory");
      const v2u f = half8(row8(fold(gb, fold(ga))));
      if (__all(f.x + 2 * tag == ca.x + cb.x && f.y == (ca.y ^ cb.y))) break;
      __builtin_amdgcn_s_sleep(1);
    }
    f32x4 y = {0.f, 0.f, 0.f, 0.f};
    y = MF16(BC(ga), BC(sel), y); y = MF16(BC(gb), BC(sel), y);
    __builtin_amdgcn_raw_buffer_store_b64(v2u{pk(y[0], y[1]), pk(y[2], y[3])}, ry, vy,
                                          4 * ((w >> 1) & 1) * 1152 + (128 * i + 64 * (hq & 1) + 32 * (w >> 2) + 16 * (w & 1)) * 2, 0);
  }
  STAMP(60);
}

extern "C" void kernel_launch(void* const* d_in, const int* in_sizes, int n_in, void* d_out, int out_size, void* d_ws, size_t ws_size,
                              hipStream_t stream) {
  gqa_fused<<<256, 512, 0, stream>>>(d_in[0], d_in[1], d_in[2], d_in[3], d_in[4], d_out, (char*)d_ws
  );
}
```

```cpp
#define DIAGK 3
#include <hip/hip_runtime.h>
#include <hip/hip_bf16.h>

typedef unsigned long long u64;
typedef unsigned u32;
typedef u32 v4u __attribute__((ext_vector_type(4)));
typedef u32 v2u __attribute__((ext_vector_type(2)));
typedef short s16x4 __attribute__((ext_vector_type(4)));
typedef short bf16x8 __attribute__((ext_vector_type(8)));
typedef float f32x16 __attribute__((ext_vector_type(16)));
typedef float f32x4 __attribute__((ext_vector_type(4)));
typedef float f32x2 __attribute__((ext_vector_type(2)));
typedef __bf16 b16x2 __attribute__((ext_vector_type(2)));
#define LAS __attribute__((address_space(3)))
#define GAS __attribute__((address_space(1)))
#define DI __device__ __forceinline__
typedef __amdgpu_buffer_rsrc_t rsrc_t;

constexpr u32 PG = 8192;
constexpr u32 SLAB = 32768;
constexpr u32 IN9 = 16384, POUT = 24576, CMAIN = 28672, C9 = 29696, CP = 30208, EPOCH = 31744;
constexpr u32 WS_BYTES = 256 * SLAB;
constexpr u32 OOB = 0x80000000u;
constexpr int SPIN_CAP = 1 << 16;
#define CODE_BYTES 17920
#define PACE 6
#define PSLEEP 1
#define KPOS 0
#define SH0 1
#define SH1 2
#define SH2 3
#define SH3 4
#define HPRIO 2
#define PPOS 3
#define GSLEEP 20

#define STAMP(i) do { if ((i) == 0) { asm volatile("s_memrealtime %0\n\ts_waitcnt lgkmcnt(0)" : "=s"(dg_t0) :: "memory"); dg_tk = dg_t0; } else if ((i) == DIAGK) { asm volatile("s_memrealtime %0\n\ts_waitcnt lgkmcnt(0)" : "=s"(dg_tk) :: "memory"); } } while (0)
#define SB() __builtin_amdgcn_sched_barrier(0)
#define HT(k, N) do { asm volatile("s_waitcnt vmcnt(%0) lgkmcnt(0)\n\ts_barrier" :: "i"(N) : "memory"); } while (0)
#define CT(k) do { SB(); asm volatile("s_waitcnt lgkmcnt(0)\n\ts_barrier" ::: "memory"); STAMP(3 * (k) + 3); SB(); } while (0)
#define CB(k) do { SB(); asm volatile("s_barrier" ::: "memory"); STAMP(3 * (k) + 3); SB(); } while (0)
#define HV(k, N) asm volatile("s_waitcnt vmcnt(%0)\n\ts_barrier" :: "i"(N) : "memory")
#define WV(N) asm volatile("s_waitcnt vmcnt(%0)" :: "i"(N) : "memory")
#define PIN(v) asm volatile("" : "+v"(v))

template <bool NT> DI void dma(u32 lds, u32 voff, rsrc_t rs, u32 soff) {
  if constexpr (NT) asm volatile("s_mov_b32 m0, %0\n\ts_nop 0\n\tbuffer_load_dwordx4 %1, %2, %3 offen nt lds" :: "s"(lds), "v"(voff), "s"(rs), "s"(soff) : "memory");
  else              asm volatile("s_mov_b32 m0, %0\n\ts_nop 0\n\tbuffer_load_dwordx4 %1, %2, %3 offen lds" :: "s"(lds), "v"(voff), "s"(rs), "s"(soff) : "memory");
}
DI void st16(v4u v, u32 voff, rsrc_t rs, u32 soff) {
  asm volatile("buffer_store_dwordx4 %0, %1, %2, %3 offen sc1\n\ts_nop 1" :: "v"(v), "v"(voff), "s"(rs), "s"(soff) : "memory");
}
DI void st8(v2u v, u32 voff, rsrc_t rs, u32 soff) {
  asm volatile("buffer_store_dwordx2 %0, %1, %2, %3 offen sc1" :: "v"(v), "v"(voff), "s"(rs), "s"(soff) : "memory");
}
DI s16x4 tr4(u32 a) { return __builtin_amdgcn_ds_read_tr16_b64_v4i16((LAS s16x4*)(size_t)a); }
DI bf16x8 trd(u32 a0, u32 a1) { return __builtin_shufflevector(tr4(a0), tr4(a1), 0, 1, 2, 3, 4, 5, 6, 7); }
DI bf16x8 rd16(u32 a) { return *(LAS bf16x8*)(size_t)a; }
DI u32 pk(float a, float b) { f32x2 v = {a, b}; return __builtin_bit_cast(u32, __builtin_convertvector(v, b16x2)); }
template <int S> DI v4u pack8(const f32x16& c) {
  v4u r = {pk(c[8 * S], c[8 * S + 1]), pk(c[8 * S + 2], c[8 * S + 3]), pk(c[8 * S + 4], c[8 * S + 5]), pk(c[8 * S + 6], c[8 * S + 7])};
  PIN(r);
  return r;
}
DI f32x16 MF32(bf16x8 a, bf16x8 b, f32x16 c) { SB(); f32x16 d = __builtin_amdgcn_mfma_f32_32x32x16_bf16(a, b, c, 0, 0, 0); PIN(d); SB(); return d; }
DI f32x4 MS16(bf16x8 a, bf16x8 b, f32x4 c) { SB(); f32x4 d = __builtin_amdgcn_mfma_f32_16x16x32_bf16(a, b, c, 0, 0, 0); PIN(d); SB(); return d; }
#define MF16(a, b, c) __builtin_amdgcn_mfma_f32_16x16x32_bf16((a), (b), (c), 0, 0, 0)
#define BC(v) __builtin_bit_cast(bf16x8, (v))
template <int C> DI float dppf(float v) { return __builtin_bit_cast(float, __builtin_amdgcn_mov_dpp(__builtin_bit_cast(int, v), C, 0xF, 0xF, true)); }
template <int C> DI u32 dppu(u32 v) { return (u32)__builtin_amdgcn_mov_dpp((int)v, C, 0xF, 0xF, true); }
DI u32 xor3(u32 a, u32 b, u32 c) { return __builtin_amdgcn_bitop3_b32(a, b, c, 0x96); }
DI v2u fold(v4u d) { return v2u{(d.x + d.y + d.z) + d.w, xor3(d.x, d.y, d.z) ^ d.w}; }
DI v2u fold(v4u d, v2u a) { return v2u{(d.x + d.y + a.x) + d.z + d.w, xor3(xor3(d.x, d.y, a.y), d.z, d.w)}; }
DI v2u row8(v2u v) { return v2u{v.x + dppu<0x128>(v.x), v.y ^ dppu<0x128>(v.y)}; }
DI v2u half8(v2u v) {
  v.x += dppu<0x141>(v.x); v.y ^= dppu<0x141>(v.y); v.x += dppu<0x4E>(v.x); v.y ^= dppu<0x4E>(v.y);
  v.x += dppu<0xB1>(v.x); v.y ^= dppu<0xB1>(v.y);
  return v;
}

__global__ void __launch_bounds__(512, 2)
gqa_fused(const void* __restrict__ x, const void* __restrict__ kc, const void* __restrict__ vc, const void* __restrict__ wq,
          const void* __restrict__ wo, void* __restrict__ out, char* __restrict__ ws
) {
  __shared__ __attribute__((aligned(1024))) char smem[20 * PG];
  u64 pc0;
  asm volatile("s_getpc_b64 %0" : "=s"(pc0));
  const u32 L = (u32)(size_t)(LAS char*)smem;
  const u32 tid = threadIdx.x, lane = tid & 63;
  const u32 w = __builtin_amdgcn_readfirstlane(tid >> 6);
  const u32 b = blockIdx.x, hq = b & 7, bp = b >> 5, i = (b >> 3) & 3, h = 4 * hq + i;
  u64 dg_t0, dg_tk;
  STAMP(0);
  const rsrc_t rw = __builtin_amdgcn_make_buffer_rsrc((void*)ws, 0, WS_BYTES, 0x00020000);
  const rsrc_t ry = __builtin_amdgcn_make_buffer_rsrc(out, 0, 512 * 1152, 0x00020000);
  const u32 me = hq * 4 + i, row = lane >> 4, col = lane & 15;
  const u32 hh = lane >> 5, l31 = lane & 31;
  const u32 ct = w & 3, bb = w >> 2, q4 = col >> 2, p4 = lane & 3;
  f32x16 zero;
#pragma unroll
  for (int n = 0; n < 16; ++n) zero[n] = 0.f;
  u32 tag, vy, vg, vgc, v16, vcm, ap, aw[2][2], av[8]; v4u sel;
  bf16x8 vf[8], bw[8]; v4u uu[4][2]; f32x16 u;
  auto setup2 = [&]() {
    const u32 one = (col & 1) ? 0x3F800000u : 0x00003F80u;
    sel = v4u{(col >> 1) == 0 ? one : 0u, (col >> 1) == 1 ? one : 0u, (col >> 1) == 2 ? one : 0u, (col >> 1) == 3 ? one : 0u};
    const u32 yrow = 64 * bp + 32 * (hq >> 2) + 16 * ((hq >> 1) & 1) + (col & 3) + 8 * ((col >> 2) & 1);
    vy = col < 8 ? yrow * 1152 + 8 * row : OOB;
    vg = row * 2048 + w * 256 + col * 16 + b * SLAB; vgc = row * 128 + w * 8 + b * SLAB + CMAIN;
    v16 = lane * 16; vcm = (col & 7) == 0 ? (col >> 3) * (2 * SLAB) + row * 8 : OOB;
    ap = L + 2 * PG + bb * 2048 + lane * 16;
    PIN(sel); PIN(vy); PIN(vg); PIN(vgc); PIN(v16); PIN(vcm); PIN(ap);
#pragma unroll
    for (int t = 0; t < 2; ++t) {
      aw[0][t] = L + 256 * (8 * hh + 4 * t + q4) + 16 * ((4 * ct + 2 * (row & 1) + (p4 >> 1)) ^ ((q4 << 2) | (2 * hh + t))) + 8 * (p4 & 1);
      aw[1][t] = aw[0][t] + 8 * PG;
      PIN(aw[0][t]); PIN(aw[1][t]);
    }
    const u32 arow = 256 * l31 + 16 * (hh ^ (((l31 & 3) << 2) | ((l31 >> 2) & 3)));
#pragma unroll
    for (int n = 0; n < 8; ++n) { av[n] = L + (16 + bb) * PG + (arow ^ (128 * (bp & 1)) ^ (32 * n)); PIN(av[n]); }
  };
  auto rV = [&](int slot, int s) { vf[s] = rd16(av[s] + 2 * slot * PG); };
  auto rW = [&](int s, int pg) { const u32 o = (pg & 7) * PG + (s & 1) * 4096; bw[s] = trd(aw[pg >> 3][0] + o, aw[pg >> 3][1] + o); };
  auto headW = [&](int hs, int p0, int p1, int p2, int p3, auto f) {
    const int pg[4] = {p0, p1, p2, p3};
    const f32x16 o = u;
    rW(0, p0); SB(); rW(1, p0); SB(); rW(2, p1); SB(); rW(3, p1); SB();
    uu[hs - 1][0] = pack8<0>(o); uu[hs - 1][1] = pack8<1>(o);
#pragma unroll
    for (int s = 0; s < 8; ++s) {
      u = MF32(vf[s], bw[s], s ? u : zero);
      if (s < 4) rW(s + 4, pg[2 + (s >> 1)]);
      if (hs == 1) rV(1, s);
      f(s);
    }
  };
  auto lagW = [&](int hs, int p0, int p1, int p2, int p3, auto f) {
    const int pg[4] = {p0, p1, p2, p3};
#pragma unroll
    for (int s = 4; s < 8; ++s) { u = MF32(vf[s], bw[s], u); rW(s - 4, pg[(s - 4) >> 1]); if (hs == 2) rV(1, s); f(s - 4); }
    const f32x16 o = u;
#pragma unroll
    for (int s = 0; s < 4; ++s) {
      u = MF32(vf[s], bw[s], s ? u : zero);
      rW(s + 4, pg[2 + (s >> 1)]);
      if (hs == 1) rV(1, s);
      if (s == 2) uu[hs - 1][0] = pack8<0>(o);
      if (s == 3) uu[hs - 1][1] = pack8<1>(o);
      f(s + 4);
    }
  };
  auto nop = [](int) {};
  asm volatile("" :: "s"(x), "s"(kc), "s"(vc), "s"(wq), "s"(wo), "s"(out), "s"(ws));

  if (__builtin_expect(w >= 4, 1)) {
    const u32 g = w - 4;
    const rsrc_t rx = __builtin_amdgcn_make_buffer_rsrc((void*)x, 0, 512 * 1152, 0x00020000);
    const rsrc_t rk = __builtin_amdgcn_make_buffer_rsrc((void*)kc, 0, 256 * 8192, 0x00020000);
    const rsrc_t rv = __builtin_amdgcn_make_buffer_rsrc((void*)vc, 0, 256 * 8192, 0x00020000);
    const rsrc_t rq = __builtin_amdgcn_make_buffer_rsrc((void*)wq, 0, 576 * 8192, 0x00020000);
    const rsrc_t ro = __builtin_amdgcn_make_buffer_rsrc((void*)wo, 0, 4096 * 1152, 0x00020000);
    const u32 xr = 8 * g + (lane >> 3);
    const u32 vx = xr * 1152 + 16 * ((lane & 7) ^ ((xr >> 1) & 7));
    const u32 pr = 4 * g + row;
    const u32 pc = col ^ ((row << 2) | g);
    const u32 vq = pr * 8192 + 16 * pc;
    const u32 Lg = L + 1024 * g;
    const u32 r0 = 3 * ((bp + i) % 3);
    const u32 sx = bp * (64 * 1152), sq = 256 * h;
    auto step = [&](int s) {
      u32 ks = r0 + s; ks = ks >= 9 ? ks - 9 : ks;
      const u32 o = sx + ks * 128, o2 = sq + ks * (64 * 8192);
      dma<false>(Lg + (8 + (s & 3)) * PG, vx, rx, o); dma<false>(Lg + (8 + (s & 3)) * PG + 4096, vx, rx, o + 32 * 1152);
#pragma unroll
      for (int n = 0; n < 4; ++n) dma<true>(Lg + 2 * (s & 3) * PG + n * 4096, vq, rq, o2 + n * (16 * 8192));
    };
    step(0); step(1);
    {
      const rsrc_t rc = __builtin_amdgcn_make_buffer_rsrc((void*)(pc0 & ~255ull), 0, CODE_BYTES, 0x00020000);
      asm volatile("s_buffer_load_dword s100, %0, %1\n\ts_buffer_load_dword s101, %2, %3\n\ts_buffer_load_dword s101, %2, %3 offset:64\n\t"
                   "s_buffer_load_dword s101, %2, %3 offset:128\n\ts_buffer_load_dword s101, %4, %5"
                   :: "s"(rw), "s"(bp * (32 * SLAB) + EPOCH), "s"(rc), "s"(((b >> 3) * 4 + g) * 192), "s"(ry), "s"(bp * (64 * 1152)) : "s100", "s101", "memory");
    }
    u32 vkv = pr * 256 + 16 * pc, vo = pr * 1152 + 16 * pc, vo9 = (pr + 64 * (pc >> 3)) * 1152 + 16 * (pc & 7);
    u32 pl = L + 2 * PG + g * 4096 + row * 512 + col * 16;
    u32 vcl = row * 8;
    PIN(vkv); PIN(vo); PIN(vo9); PIN(pl); PIN(vcl);
    u32 bs = b;
    asm volatile("" : "+s"(bs));
    setup2();
    auto ldk = [&](u32 n) {
      const u32 o = ((2 * bp + n) * 16 + (h >> 1)) * 8192;
      dma<true>(Lg + (12 + n) * PG, vkv, rk, o); dma<true>(Lg + (12 + n) * PG + 4096, vkv, rk, o + 4096);
    };
    auto ldv = [&](u32 n) {
      const u32 o = ((2 * bp + (n & 1)) * 16 + 2 * hq + ((n >> 1) ^ (bp >> 2))) * 8192;
      dma<true>(Lg + (16 + n) * PG, vkv, rv, o); dma<true>(Lg + (16 + n) * PG + 4096, vkv, rv, o + 4096);
    };
    auto ldw1 = [&](int n, int pg, int k) {
      dma<true>(Lg + pg * PG + k * 4096, vo, ro, ((bs & 7) * 512 + 32 * (n ^ ((bs >> 5) << 1)) + 16 * k) * 1152 + 256 * ((bs >> 3) & 3));
    };
    auto ldw = [&](int n, int pg) { ldw1(n, pg, 0); ldw1(n, pg, 1); };
    auto ldn = [&](int n, int pg) {
      const u32 o = (h * 128 + 32 * n) * 1152 + 1024;
      dma<true>(Lg + pg * PG, vo9, ro, o); dma<true>(Lg + pg * PG + 4096, vo9, ro, o + 16 * 1152);
    };
    auto extra = [&](int t) {
      if (t == KPOS) { ldk(i & 1); ldk((i & 1) ^ 1); }
      const u32 grp = bp & 3;
      if ((t == SH0 && grp == 0) || (t == SH1 && grp == 1) || (t == SH2 && grp == 2) || (t == SH3 && grp == 3)) {
        ldv(i); ldw(0, 14); ldw(1, 15);
        dma<true>(Lg + (16 + (i ^ 1)) * PG, vo9, ro, (h * 128 + 16 * (bp & 3)) * 1152 + 1024);
      }
    };
    step(2);
    HV(0, 12); step(3); extra(0);
    HV(1, 12); step(4); extra(1);
    HV(2, 12); step(5); extra(2);
    HV(3, 12); step(6); extra(3);
    HV(4, 12); step(7); extra(4);
    HV(5, 12); step(8);
    HV(6, 12); ldv(i ^ 1); ldv(i ^ 2);
    HV(7, 10); ldv(i ^ 3);
    asm volatile("s_waitcnt lgkmcnt(0)\n\ts_mov_b32 %0, s100" : "=s"(tag) :: "memory");
    tag += 1;
    HV(8, 8);  ldw(2, 9);
    HV(9, 8);  WV(PACE); ldw(3, 10); WV(PACE); ldn(0, 2); WV(PACE); ldn(1, 3);
    HV(10, 8);
    rV(0, 0); rW(0, 14); SB(); rV(0, 1); rW(1, 14); SB(); rV(0, 2); rW(2, 15); SB(); rV(0, 3); rW(3, 15);
#pragma unroll
    for (int s = 0; s < 4; ++s) { u = MF32(vf[s], bw[s], s ? u : zero); rV(0, s + 4); }
    WV(PACE); ldw(4, 4); WV(PACE); ldw(5, 5); WV(PACE); ldw(6, 6); WV(PACE); ldw(7, 7);
    HV(11, 8);
    rW(4, 9); SB(); rW(5, 9); SB(); rW(6, 10); SB(); rW(7, 10); SB();
    ldw(8, 11);
#pragma unroll
    for (int s = 4; s < 8; ++s) u = MF32(vf[s], bw[s], u);
    WV(PACE); ldw(9, 0); WV(PACE); ldw(10, 1); WV(PACE); ldw(11, 8);
    __builtin_amdgcn_s_setprio(HPRIO);
    HV(12, 8);
    headW(1, 4, 5, 6, 7, [&](int s) { ldw1(12 + (s >> 1), 12 + (s >> 1), s & 1); });
    HV(13, 8);
    v4u p0, p1, p2, p3; v2u c0, c1, c2, c3;
    const u32 spl = ((bp << 5) | ((g ^ (bp >> 1)) << 3) | hq) * SLAB;
#define LOAD_P "buffer_load_dwordx4 %0, %8, %10, %11 offen sc1\n\tbuffer_load_dwordx4 %1, %8, %10, %11 offen offset:1024 sc1\n\t" \
               "buffer_load_dwordx4 %2, %8, %10, %11 offen offset:2048 sc1\n\tbuffer_load_dwordx4 %3, %8, %10, %11 offen offset:3072 sc1\n\t" \
               "buffer_load_dwordx2 %4, %9, %10, %12 offen sc1\n\tbuffer_load_dwordx2 %5, %9, %10, %12 offen offset:32 sc1\n\t" \
               "buffer_load_dwordx2 %6, %9, %10, %12 offen offset:64 sc1\n\tbuffer_load_dwordx2 %7, %9, %10, %12 offen offset:96 sc1"
#define P_ARGS "=&v"(p0), "=&v"(p1), "=&v"(p2), "=&v"(p3), "=&v"(c0), "=&v"(c1), "=&v"(c2), "=&v"(c3) : "v"(v16), "v"(vcl), "s"(rw), "s"(spl + POUT), "s"(spl + CP) : "memory"
    headW(2, 11, 0, 1, 8, [&](int s) { if (s == PPOS) asm volatile(LOAD_P : P_ARGS); });
    HV(14, 8);
    headW(3, 12, 13, 14, 15, nop);
    asm volatile("s_waitcnt vmcnt(0)" : "+v"(p0), "+v"(p1), "+v"(p2), "+v"(p3), "+v"(c0), "+v"(c1), "+v"(c2), "+v"(c3) :: "memory");
    STAMP(62);
    _Pragma("nounroll") for (int sp = 0; sp < SPIN_CAP; ++sp) {
      *(LAS v4u*)(size_t)pl = p0; *(LAS v4u*)(size_t)(pl + 256) = p1; *(LAS v4u*)(size_t)(pl + 2048) = p2; *(LAS v4u*)(size_t)(pl + 2304) = p3;
      const v2u f0 = fold(p1, fold(p0)), f1 = fold(p3, fold(p2));
      const v2u f = half8(row8(v2u{f0.x + f1.x, f0.y ^ f1.y}));
      if (__all(f.x + 4 * tag == (c0.x + c1.x) + (c2.x + c3.x) && f.y == (xor3(c0.y, c1.y, c2.y) ^ c3.y))) break;
      asm volatile("s_waitcnt lgkmcnt(0)\n\t" LOAD_P "\n\ts_waitcnt vmcnt(0)" : P_ARGS);
    }
    HT(15, 0);
  } else {
    setup2();
    const u32 mt = w >> 1;
    u32 aq[8][2], ax[2], ak[8], a9[4][2];
    const u32 xrow = 16 * w + col, key = 16 * (col >> 3) + (col & 7);
    ax[0] = L + 8 * PG + 128 * xrow + 16 * (row ^ ((xrow >> 1) & 7)); ax[1] = ax[0] ^ 64;
    PIN(ax[0]); PIN(ax[1]);
#pragma unroll
    for (int n = 0; n < 8; ++n) {
#pragma unroll
      for (int t = 0; t < 2; ++t) {
        aq[n][t] = L + 256 * (8 * row + 4 * t + q4) + 16 * ((2 * n + (p4 >> 1)) ^ ((q4 << 2) | ((2 * row + t) & 3))) + 8 * (p4 & 1);
        PIN(aq[n][t]);
      }
      ak[n] = L + (12 + mt) * PG + 256 * key + 16 * ((2 * n + (row >> 1)) ^ (((key & 3) << 2) | ((key >> 2) & 3))) + 8 * (row & 1);
      a9[n >> 1][n & 1] = L + (16 + mt + 2 * ((i >> 1) ^ (bp >> 2))) * PG + 256 * (16 * (row >> 1) + 4 * (row & 1) + 8 * (n & 1) + q4) +
                          16 * ((4 * (n >> 1) + p4) ^ ((q4 << 2) | ((row & 1) + 2 * (n & 1))));
      PIN(ak[n]); PIN(a9[n >> 1][n & 1]);
    }
    u32 v9 = (row << 3) * SLAB + col * 16, vc9 = (col & 7) == 0 ? (row << 3) * SLAB + (col >> 3) * SLAB : OOB, vcs = col == 0 ? row * 8 : OOB;
    PIN(v9); PIN(vc9); PIN(vcs);
    const u32 sp0 = b * SLAB + POUT + w * 1024, sp1 = b * SLAB + CP + w * 32;
    const u32 s9 = ((bp << 5) | (2 * w)) * SLAB;
    const u32 s9a = s9 + IN9 + me * 256, s9b = s9a + SLAB, s9c = s9 + C9 + me * 8;
    asm volatile("" :: "s"(sp0), "s"(sp1), "s"(s9a), "s"(s9b), "s"(s9c));

    const f32x4 z4 = {0.f, 0.f, 0.f, 0.f};
    f32x4 qa[8];
#pragma unroll
    for (int n = 0; n < 8; ++n) qa[n] = z4;
    bf16x8 A[16], B[2], kf[2][4];
    auto rA = [&](int sl, int n) { const u32 o = (2 * sl + (n >> 3)) * PG; A[n] = trd(aq[n & 7][0] + o, aq[n & 7][1] + o); };
    auto rB = [&](int sl, int k) { B[k] = rd16(ax[k] + sl * PG); };
    auto rK = [&](int T, int ks) {
      const s16x4 lo = *(LAS s16x4*)(size_t)(ak[2 * ks + T] + T * 2048), hi = *(LAS s16x4*)(size_t)(ak[2 * ks + 1 - T] + T * 2048);
      kf[T][ks] = __builtin_shufflevector(lo, hi, 0, 1, 2, 3, 4, 5, 6, 7);
    };
    bf16x8 Bc;
    auto head = [&](int sl, bool carry) {
      rB(sl, 0);
#pragma unroll
      for (int n = 0; n < 6; ++n) { rA(sl, n); if (carry) qa[n + 2] = MS16(A[n + 10], Bc, qa[n + 2]); else SB(); }
      rB(sl, 1);
    };
    auto p1 = [&](int t) {
      head(t & 3, t > 0);
#pragma unroll
      for (int n = 0; n < 10; ++n) { qa[n & 7] = MS16(A[n], B[n >> 3], qa[n & 7]); rA(t & 3, n + 6); }
      Bc = B[1];
      if (t == KPOS + 4) { rK(0, 0); rK(0, 1); rK(0, 2); rK(0, 3); }
      if (t == KPOS + 5) { rK(1, 0); rK(1, 1); rK(1, 2); rK(1, 3); }
    };
    CT(0); p1(0); CT(1); p1(1); CT(2); p1(2); CT(3); p1(3);
    asm volatile("s_buffer_load_dword %0, %1, %2\n\ts_waitcnt lgkmcnt(0)" : "=s"(tag) : "s"(rw), "s"(bp * (32 * SLAB) + EPOCH) : "memory");
    tag += 1;
    CT(4); p1(4); CT(5); p1(5); CB(6); p1(6); CB(7); p1(7);
    CB(8);
    head(0, true);
#pragma unroll
    for (int n = 0; n < 8; ++n) { qa[n] = MS16(A[n], B[0], qa[n]); if (n < 2) rA(0, n + 6); }
    CB(9);
    f32x4 sa = z4, sb = z4;
    {
      v4u q[4];
      auto pq = [&](int ks) { q[ks] = v4u{pk(qa[2 * ks][0], qa[2 * ks][1]), pk(qa[2 * ks][2], qa[2 * ks][3]), pk(qa[2 * ks + 1][0], qa[2 * ks + 1][1]), pk(qa[2 * ks + 1][2], qa[2 * ks + 1][3])}; PIN(q[ks]); };
#pragma unroll
      for (int n = 8; n < 14; ++n) { rA(0, n); SB(); }
      qa[0] = MS16(A[8], B[1], qa[0]); rA(0, 14);
      qa[1] = MS16(A[9], B[1], qa[1]); rA(0, 15);
      qa[2] = MS16(A[10], B[1], qa[2]);
      qa[3] = MS16(A[11], B[1], qa[3]);
      qa[4] = MS16(A[12], B[1], qa[4]);
      qa[5] = MS16(A[13], B[1], qa[5]);
      pq(0);
      qa[6] = MS16(A[14], B[1], qa[6]);
      qa[7] = MS16(A[15], B[1], qa[7]);
      pq(1);
      sa = MS16(kf[0][0], BC(q[0]), sa); sb = MS16(kf[1][0], BC(q[0]), sb);
      pq(2);
      sa = MS16(kf[0][1], BC(q[1]), sa); sb = MS16(kf[1][1], BC(q[1]), sb);
      pq(3);
      sa = MS16(kf[0][2], BC(q[2]), sa); sb = MS16(kf[1][2], BC(q[2]), sb);
      sa = MS16(kf[0][3], BC(q[3]), sa); sb = MS16(kf[1][3], BC(q[3]), sb);
    }
    CB(10);
    {
      bf16x8 vt[8], wt[16]; f32x4 ot[8], y[4]; v4u of[4];
      auto rT = [&](int n) { vt[n] = trd(a9[n >> 1][0] + 8 * (n & 1), a9[n >> 1][1] + 8 * (n & 1)); };
      auto rN = [&](int n) { const int ks = n >> 2, c = 4 * (ks >> 1) + (n & 3); wt[n] = trd(aq[c][0] + (2 + (ks & 1)) * PG, aq[c][1] + (2 + (ks & 1)) * PG); };
      rT(0); rT(1); rT(2); rT(3); SB();
      float mx, m2;
      asm volatile("s_nop 1\n\tv_max3_f32 %0, %2, %3, %4\n\tv_max3_f32 %1, %5, %6, %7\n\tv_max3_f32 %0, %0, %8, %9\n\tv_max_f32 %0, %0, %1\n\t"
                   "v_mov_b32 %1, %0\n\ts_nop 1\n\tv_permlane16_swap_b32 %0, %1\n\tv_max_f32 %0, %0, %1\n\t"
                   "v_mov_b32 %1, %0\n\ts_nop 1\n\tv_permlane32_swap_b32 %0, %1\n\tv_max_f32 %0, %0, %1"
                   : "=&v"(mx), "=&v"(m2) : "v"(sa[0]), "v"(sa[1]), "v"(sa[2]), "v"(sa[3]), "v"(sb[0]), "v"(sb[1]), "v"(sb[2]), "v"(sb[3]));
      const float c = 0.08838834764831845f * 1.4426950408889634f, mc = -mx * c;
      float e[8], z2;
#pragma unroll
      for (int n = 0; n < 4; ++n) { e[n] = __builtin_amdgcn_exp2f(__builtin_fmaf(sa[n], c, mc)); e[4 + n] = __builtin_amdgcn_exp2f(__builtin_fmaf(sb[n], c, mc)); }
      float z = ((e[0] + e[2]) + (e[4] + e[6])) + ((e[1] + e[3]) + (e[5] + e[7]));
      asm volatile("v_mov_b32 %1, %0\n\ts_nop 1\n\tv_permlane16_swap_b32 %0, %1\n\tv_add_f32 %0, %0, %1\n\t"
                   "v_mov_b32 %1, %0\n\ts_nop 1\n\tv_permlane32_swap_b32 %0, %1\n\tv_add_f32 %0, %0, %1" : "+v"(z), "=&v"(z2));
      const float r = __builtin_amdgcn_rcpf(z);
      const v4u pv = {pk(e[0] * r, e[1] * r), pk(e[2] * r, e[3] * r), pk(e[4] * r, e[5] * r), pk(e[6] * r, e[7] * r)};
      st16(pv, v16, rw, sp0);
      rT(4); rT(5); rT(6); rT(7);
      v2u ck;
#pragma unroll
      for (int n = 0; n < 8; ++n) {
        ot[n] = MS16(vt[n], BC(pv), z4);
        if (n == 0) { ck = row8(fold(pv)); PIN(ck); } else if (n == 1) { ck = half8(ck); ck.x += tag; st8(ck, vcs, rw, sp1); }
        else if (n >= 4) { rV(0, n - 4); rW(n - 4, 14 + ((n - 4) >> 1)); }
      }
      STAMP(61);
      CB(11);
#pragma unroll
      for (int s = 0; s < 4; ++s) {
        rN(2 * s); rN(2 * s + 1);
        of[s] = v4u{pk(ot[2 * s][0], ot[2 * s][1]), pk(ot[2 * s][2], ot[2 * s][3]), pk(ot[2 * s + 1][0], ot[2 * s + 1][1]), pk(ot[2 * s + 1][2], ot[2 * s + 1][3])};
        PIN(of[s]);
        u = MF32(vf[s], bw[s], s ? u : zero);
      }
#pragma unroll
      for (int n = 0; n < 16; ++n) {
        y[n & 3] = MS16(wt[n], BC(of[n >> 2]), n < 4 ? z4 : y[n & 3]);
        if (n < 8) rN(n + 8); else if (n & 1) rW(4 + ((n - 8) >> 1), 9 + ((n - 8) >> 2)); else rV(0, 4 + ((n - 8) >> 1));
      }
      v4u e0, e1; v2u g0, g1;
      CB(12);
      lagW(1, 4, 5, 6, 7, [&](int s) {
        if (s == 0) { e0 = v4u{pk(y[0][0], y[0][1]), pk(y[0][2], y[0][3]), pk(y[1][0], y[1][1]), pk(y[1][2], y[1][3])}; st16(e0, v9, rw, s9a); }
        else if (s == 1) { e1 = v4u{pk(y[2][0], y[2][1]), pk(y[2][2], y[2][3]), pk(y[3][0], y[3][1]), pk(y[3][2], y[3][3])}; st16(e1, v9, rw, s9b); }
        else if (s == 2) { g0 = fold(e0); PIN(g0); } else if (s == 3) { g1 = fold(e1); PIN(g1); }
        else if (s == 4) { g0 = row8(g0); g1 = row8(g1); g0 = col < 8 ? g0 : g1; PIN(g0); }
        else if (s == 5) { g0 = half8(g0); g0.x += tag; st8(g0, vc9, rw, s9c); } });
    }
    CB(13); lagW(2, 11, 0, 1, 8, nop);
    CB(14); lagW(3, 12, 13, 14, 15, nop);
#pragma unroll
    for (int s = 4; s < 8; ++s) u = MF32(vf[s], bw[s], u);
    CB(15);
  }
  {
    const u32 so = ((bp << 5) | (i << 3) | (bb * 4 + (ct >> 1))) * SLAB;
    const u32 sd0 = __builtin_amdgcn_readfirstlane(so + hq * 2048 + (ct & 1) * 1024), sd1 = sd0 + 2 * SLAB;
    const u32 sc0 = __builtin_amdgcn_readfirstlane(so + CMAIN + hq * 128 + (ct & 1) * 32);
    bf16x8 pf[8];
#pragma unroll
    for (int n = 0; n < 8; ++n) { pf[n] = rd16(ap + (n >> 1) * 4096 + (n & 1) * 1024); SB(); }
    uu[3][0] = pack8<0>(u); uu[3][1] = pack8<1>(u);
    f32x16 o = zero;
#pragma unroll
    for (int n = 0; n < 8; ++n) o = MF32(pf[n], BC(uu[n >> 1][n & 1]), o);
    const v4u d0 = pack8<0>(o); st16(d0, v16, rw, sd0);
    const v4u d1 = pack8<1>(o); st16(d1, v16, rw, sd1);
    const v2u f0 = row8(fold(d0)), f1 = row8(fold(d1));
    v2u f = half8(col < 8 ? f0 : f1); f.x += tag; st8(f, vcm, rw, sc0);
  }
  STAMP(58);
  if (w == 7) {
    const u32 vd = v16 + b * SLAB + IN9, vc = row * 8 + b * SLAB + C9;
    v4u d[8]; v2u c[8];
    _Pragma("nounroll") for (int sp = 0; sp < SPIN_CAP; ++sp) {
      asm volatile("buffer_load_dwordx4 %0, %16, %18, 0 offen sc1\n\tbuffer_load_dwordx4 %1, %16, %18, 0 offen offset:1024 sc1\n\t"
                   "buffer_load_dwordx4 %2, %16, %18, 0 offen offset:2048 sc1\n\tbuffer_load_dwordx4 %3, %16, %18, 0 offen offset:3072 sc1\n\t"
                   "buffer_load_dwordx4 %4, %16, %18, %19 offen sc1\n\tbuffer_load_dwordx4 %5, %16, %18, %19 offen offset:1024 sc1\n\t"
                   "buffer_load_dwordx4 %6, %16, %18, %19 offen offset:2048 sc1\n\tbuffer_load_dwordx4 %7, %16, %18, %19 offen offset:3072 sc1\n\t"
                   "buffer_load_dwordx2 %8, %17, %18, 0 offen sc1\n\tbuffer_load_dwordx2 %9, %17, %18, 0 offen offset:32 sc1\n\t"
                   "buffer_load_dwordx2 %10, %17, %18, 0 offen offset:64 sc1\n\tbuffer_load_dwordx2 %11, %17, %18, 0 offen offset:96 sc1\n\t"
                   "buffer_load_dwordx2 %12, %17, %18, 0 offen offset:128 sc1\n\tbuffer_load_dwordx2 %13, %17, %18, 0 offen offset:160 sc1\n\t"
                   "buffer_load_dwordx2 %14, %17, %18, 0 offen offset:192 sc1\n\tbuffer_load_dwordx2 %15, %17, %18, 0 offen offset:224 sc1\n\t"
                   "s_waitcnt vmcnt(0)"
                   : "=&v"(d[0]), "=&v"(d[1]), "=&v"(d[2]), "=&v"(d[3]), "=&v"(d[4]), "=&v"(d[5]), "=&v"(d[6]), "=&v"(d[7]),
                     "=&v"(c[0]), "=&v"(c[1]), "=&v"(c[2]), "=&v"(c[3]), "=&v"(c[4]), "=&v"(c[5]), "=&v"(c[6]), "=&v"(c[7])
                   : "v"(vd), "v"(vc), "s"(rw), "s"(4096) : "memory");
      const v2u f0 = fold(d[3], fold(d[2], fold(d[1], fold(d[0])))), f1 = fold(d[7], fold(d[6], fold(d[5], fold(d[4]))));
      const v2u f = half8(row8(v2u{f0.x + f1.x, f0.y ^ f1.y}));
      u32 cs = 0, cx = 0;
#pragma unroll
      for (int n = 0; n < 8; ++n) { cs += c[n].x; cx ^= c[n].y; }
      if (__all(f.x + 8 * tag == cs && f.y == cx)) break;
      __builtin_amdgcn_s_sleep(2);
    }
    f32x4 y = {0.f, 0.f, 0.f, 0.f};
#pragma unroll
    for (int n = 0; n < 8; ++n) y = MF16(BC(sel), BC(d[n]), y);
    __builtin_amdgcn_raw_buffer_store_b64(v2u{pk(y[0], y[1]), pk(y[2], y[3])}, ry,
                                          row < 2 ? (64 * bp + 16 * (hq >> 1) + col) * 1152 + row * 32 + (512 + 32 * (hq & 1) + 4 * i) * 2 : OOB, 0, 0);
    if (me == 0) __builtin_amdgcn_raw_buffer_store_b32(tag, rw, lane == 0 ? bp * (32 * SLAB) + EPOCH : OOB, 0, 16);
  } else {
    asm volatile("s_waitcnt vmcnt(0)" ::: "memory");
  }
  STAMP(59);
  {
    v4u ga, gb; v2u ca, cb;
    _Pragma("nounroll") for (int sp = 0; sp < SPIN_CAP; ++sp) {
      asm volatile("buffer_load_dwordx4 %0, %4, %7, 0 offen sc1\n\tbuffer_load_dwordx4 %1, %4, %7, %5 offen sc1\n\t"
                   "buffer_load_dwordx2 %2, %6, %7, 0 offen sc1\n\tbuffer_load_dwordx2 %3, %6, %7, 0 offen offset:512 sc1\n\ts_waitcnt vmcnt(0)"
                   : "=&v"(ga), "=&v"(gb), "=&v"(ca), "=&v"(cb) : "v"(vg), "s"(8192), "v"(vgc), "s"(rw) : "memory");
      const v2u f = half8(row8(fold(gb, fold(ga))));
      if (__all(f.x + 2 * tag == ca.x + cb.x && f.y == (ca.y ^ cb.y))) break;
      __builtin_amdgcn_s_sleep(1);
    }
    f32x4 y = {0.f, 0.f, 0.f, 0.f};
    y = MF16(BC(ga), BC(sel), y); y = MF16(BC(gb), BC(sel), y);
    __builtin_amdgcn_raw_buffer_store_b64(v2u{pk(y[0], y[1]), pk(y[2], y[3])}, ry, vy,
                                          4 * ((w >> 1) & 1) * 1152 + (128 * i + 64 * (hq & 1) + 32 * (w >> 2) + 16 * (w & 1)) * 2, 0);
  }
  STAMP(60);
  { const u64 until = dg_t0 + 4000 + 4 * (dg_tk - dg_t0); while (__builtin_amdgcn_s_memrealtime() < until && __builtin_amdgcn_s_memrealtime() < dg_t0 + 20000) __builtin_amdgcn_s_sleep(1); }
}

extern "C" void kernel_launch(void* const* d_in, const int* in_sizes, int n_in, void* d_out, int out_size, void* d_ws, size_t ws_size,
                              hipStream_t stream) {
  gqa_fused<<<256, 512, 0, stream>>>(d_in[0], d_in[1], d_in[2], d_in[3], d_in[4], d_out, (char*)d_ws
  );
}
```

```cpp
#include <hip/hip_runtime.h>
#include <hip/hip_bf16.h>

typedef unsigned long long u64;
typedef unsigned u32;
typedef u32 v4u __attribute__((ext_vector_type(4)));
typedef u32 v2u __attribute__((ext_vector_type(2)));
typedef short s16x4 __attribute__((ext_vector_type(4)));
typedef short bf16x8 __attribute__((ext_vector_type(8)));
typedef float f32x16 __attribute__((ext_vector_type(16)));
typedef float f32x4 __attribute__((ext_vector_type(4)));
typedef float f32x2 __attribute__((ext_vector_type(2)));
typedef __bf16 b16x2 __attribute__((ext_vector_type(2)));
#define LAS __attribute__((address_space(3)))
#define GAS __attribute__((address_space(1)))
#define DI __device__ __forceinline__
typedef __amdgpu_buffer_rsrc_t rsrc_t;

constexpr u32 PG = 8192;
constexpr u32 SLAB = 32768;
constexpr u32 IN9 = 16384, POUT = 24576, CMAIN = 28672, C9 = 29696, CP = 30208, EPOCH = 31744;
constexpr u32 WS_BYTES = 256 * SLAB;
constexpr u32 OOB = 0x80000000u;
constexpr int SPIN_CAP = 1 << 16;
#define CODE_BYTES 17920
#define PACE 6
#define PSLEEP 1
#define KPOS 0
#define SH0 1
#define SH1 2
#define SH2 3
#define SH3 4
#define HPRIO 2
#define PPOS 3
#define GSLEEP 20

#define STAMP(i)
#define SB() __builtin_amdgcn_sched_barrier(0)
#define HT(k, N) asm volatile("s_waitcnt vmcnt(%0) lgkmcnt(0)\n\ts_barrier" :: "i"(N) : "memory")
#define CT(k) do { SB(); asm volatile("s_waitcnt lgkmcnt(0)\n\ts_barrier" ::: "memory"); SB(); } while (0)
#define CB(k) do { SB(); asm volatile("s_barrier" ::: "memory"); SB(); } while (0)
#define HV(k, N) asm volatile("s_waitcnt vmcnt(%0)\n\ts_barrier" :: "i"(N) : "memory")
#define WV(N) asm volatile("s_waitcnt vmcnt(%0)" :: "i"(N) : "memory")
#define PIN(v) asm volatile("" : "+v"(v))

template <bool NT> DI void dma(u32 lds, u32 voff, rsrc_t rs, u32 soff) {
  if constexpr (NT) asm volatile("s_mov_b32 m0, %0\n\ts_nop 0\n\tbuffer_load_dwordx4 %1, %2, %3 offen nt lds" :: "s"(lds), "v"(voff), "s"(rs), "s"(soff) : "memory");
  else              asm volatile("s_mov_b32 m0, %0\n\ts_nop 0\n\tbuffer_load_dwordx4 %1, %2, %3 offen lds" :: "s"(lds), "v"(voff), "s"(rs), "s"(soff) : "memory");
}
DI void st16(v4u v, u32 voff, rsrc_t rs, u32 soff) {
  asm volatile("buffer_store_dwordx4 %0, %1, %2, %3 offen sc1\n\ts_nop 1" :: "v"(v), "v"(voff), "s"(rs), "s"(soff) : "memory");
}
DI void st8(v2u v, u32 voff, rsrc_t rs, u32 soff) {
  asm volatile("buffer_store_dwordx2 %0, %1, %2, %3 offen sc1" :: "v"(v), "v"(voff), "s"(rs), "s"(soff) : "memory");
}
DI s16x4 tr4(u32 a) { return __builtin_amdgcn_ds_read_tr16_b64_v4i16((LAS s16x4*)(size_t)a); }
DI bf16x8 trd(u32 a0, u32 a1) { return __builtin_shufflevector(tr4(a0), tr4(a1), 0, 1, 2, 3, 4, 5, 6, 7); }
DI bf16x8 rd16(u32 a) { return *(LAS bf16x8*)(size_t)a; }
DI u32 pk(float a, float b) { f32x2 v = {a, b}; return __builtin_bit_cast(u32, __builtin_convertvector(v, b16x2)); }
template <int S> DI v4u pack8(const f32x16& c) {
  v4u r = {pk(c[8 * S], c[8 * S + 1]), pk(c[8 * S + 2], c[8 * S + 3]), pk(c[8 * S + 4], c[8 * S + 5]), pk(c[8 * S + 6], c[8 * S + 7])};
  PIN(r);
  return r;
}
DI f32x16 MF32(bf16x8 a, bf16x8 b, f32x16 c) { SB(); f32x16 d = __builtin_amdgcn_mfma_f32_32x32x16_bf16(a, b, c, 0, 0, 0); PIN(d); SB(); return d; }
DI f32x4 MS16(bf16x8 a, bf16x8 b, f32x4 c) { SB(); f32x4 d = __builtin_amdgcn_mfma_f32_16x16x32_bf16(a, b, c, 0, 0, 0); PIN(d); SB(); return d; }
#define MF16(a, b, c) __builtin_amdgcn_mfma_f32_16x16x32_bf16((a), (b), (c), 0, 0, 0)
#define BC(v) __builtin_bit_cast(bf16x8, (v))
template <int C> DI float dppf(float v) { return __builtin_bit_cast(float, __builtin_amdgcn_mov_dpp(__builtin_bit_cast(int, v), C, 0xF, 0xF, true)); }
template <int C> DI u32 dppu(u32 v) { return (u32)__builtin_amdgcn_mov_dpp((int)v, C, 0xF, 0xF, true); }
DI u32 xor3(u32 a, u32 b, u32 c) { return __builtin_amdgcn_bitop3_b32(a, b, c, 0x96); }
DI v2u fold(v4u d) { return v2u{(d.x + d.y + d.z) + d.w, xor3(d.x, d.y, d.z) ^ d.w}; }
DI v2u fold(v4u d, v2u a) { return v2u{(d.x + d.y + a.x) + d.z + d.w, xor3(xor3(d.x, d.y, a.y), d.z, d.w)}; }
DI v2u row8(v2u v) { return v2u{v.x + dppu<0x128>(v.x), v.y ^ dppu<0x128>(v.y)}; }
DI v2u half8(v2u v) {
  v.x += dppu<0x141>(v.x); v.y ^= dppu<0x141>(v.y); v.x += dppu<0x4E>(v.x); v.y ^= dppu<0x4E>(v.y);
  v.x += dppu<0xB1>(v.x); v.y ^= dppu<0xB1>(v.y);
  return v;
}

__global__ void __launch_bounds__(512, 2)
gqa_fused(const void* __restrict__ x, const void* __restrict__ kc, const void* __restrict__ vc, const void* __restrict__ wq,
          const void* __restrict__ wo, void* __restrict__ out, char* __restrict__ ws
) {
  __shared__ __attribute__((aligned(1024))) char smem[20 * PG];
  u64 pc0;
  asm volatile("s_getpc_b64 %0" : "=s"(pc0));
  const u32 L = (u32)(size_t)(LAS char*)smem;
  const u32 tid = threadIdx.x, lane = tid & 63;
  const u32 w = __builtin_amdgcn_readfirstlane(tid >> 6);
  const u32 b = blockIdx.x, hq = b & 7, bp = b >> 5, i = (b >> 3) & 3, h = 4 * hq + i;
  STAMP(0);
  const rsrc_t rw = __builtin_amdgcn_make_buffer_rsrc((void*)ws, 0, WS_BYTES, 0x00020000);
  const rsrc_t ry = __builtin_amdgcn_make_buffer_rsrc(out, 0, 512 * 1152, 0x00020000);
  const u32 me = hq * 4 + i, row = lane >> 4, col = lane & 15;
  const u32 hh = lane >> 5, l31 = lane & 31;
  const u32 ct = w & 3, bb = w >> 2, q4 = col >> 2, p4 = lane & 3;
  f32x16 zero;
#pragma unroll
  for (int n = 0; n < 16; ++n) zero[n] = 0.f;
  u32 tag, vy, vg, vgc, v16, vcm, ap, aw[2][2], av[8]; v4u sel;
  bf16x8 vf[8], bw[8]; v4u uu[4][2]; f32x16 u;
  auto setup2 = [&]() {
    const u32 one = (col & 1) ? 0x3F800000u : 0x00003F80u;
    sel = v4u{(col >> 1) == 0 ? one : 0u, (col >> 1) == 1 ? one : 0u, (col >> 1) == 2 ? one : 0u, (col >> 1) == 3 ? one : 0u};
    const u32 yrow = 64 * bp + 32 * (hq >> 2) + 16 * ((hq >> 1) & 1) + (col & 3) + 8 * ((col >> 2) & 1);
    vy = col < 8 ? yrow * 1152 + 8 * row : OOB;
    vg = row * 2048 + w * 256 + col * 16 + b * SLAB; vgc = row * 128 + w * 8 + b * SLAB + CMAIN;
    v16 = lane * 16; vcm = (col & 7) == 0 ? (col >> 3) * (2 * SLAB) + row * 8 : OOB;
    ap = L + 2 * PG + bb * 2048 + lane * 16;
    PIN(sel); PIN(vy); PIN(vg); PIN(vgc); PIN(v16); PIN(vcm); PIN(ap);
#pragma unroll
    for (int t = 0; t < 2; ++t) {
      aw[0][t] = L + 256 * (8 * hh + 4 * t + q4) + 16 * ((4 * ct + 2 * (row & 1) + (p4 >> 1)) ^ ((q4 << 2) | (2 * hh + t))) + 8 * (p4 & 1);
      aw[1][t] = aw[0][t] + 8 * PG;
      PIN(aw[0][t]); PIN(aw[1][t]);
    }
    const u32 arow = 256 * l31 + 16 * (hh ^ (((l31 & 3) << 2) | ((l31 >> 2) & 3)));
#pragma unroll
    for (int n = 0; n < 8; ++n) { av[n] = L + (16 + bb) * PG + (arow ^ (128 * (bp & 1)) ^ (32 * n)); PIN(av[n]); }
  };
  auto rV = [&](int slot, int s) { vf[s] = rd16(av[s] + 2 * slot * PG); };
  auto rW = [&](int s, int pg) { const u32 o = (pg & 7) * PG + (s & 1) * 4096; bw[s] = trd(aw[pg >> 3][0] + o, aw[pg >> 3][1] + o); };
  auto headW = [&](int hs, int p0, int p1, int p2, int p3, auto f) {
    const int pg[4] = {p0, p1, p2, p3};
    const f32x16 o = u;
    rW(0, p0); SB(); rW(1, p0); SB(); rW(2, p1); SB(); rW(3, p1); SB();
    uu[hs - 1][0] = pack8<0>(o); uu[hs - 1][1] = pack8<1>(o);
#pragma unroll
    for (int s = 0; s < 8; ++s) {
      u = MF32(vf[s], bw[s], s ? u : zero);
      if (s < 4) rW(s + 4, pg[2 + (s >> 1)]);
      if (hs == 1) rV(1, s);
      f(s);
    }
  };
  auto lagW = [&](int hs, int p0, int p1, int p2, int p3, auto f) {
    const int pg[4] = {p0, p1, p2, p3};
#pragma unroll
    for (int s = 4; s < 8; ++s) { u = MF32(vf[s], bw[s], u); rW(s - 4, pg[(s - 4) >> 1]); if (hs == 2) rV(1, s); f(s - 4); }
    const f32x16 o = u;
#pragma unroll
    for (int s = 0; s < 4; ++s) {
      u = MF32(vf[s], bw[s], s ? u : zero);
      rW(s + 4, pg[2 + (s >> 1)]);
      if (hs == 1) rV(1, s);
      if (s == 2) uu[hs - 1][0] = pack8<0>(o);
      if (s == 3) uu[hs - 1][1] = pack8<1>(o);
      f(s + 4);
    }
  };
  auto nop = [](int) {};
  asm volatile("" :: "s"(x), "s"(kc), "s"(vc), "s"(wq), "s"(wo), "s"(out), "s"(ws));

  if (__builtin_expect(w >= 4, 1)) {
    const u32 g = w - 4;
    const rsrc_t rx = __builtin_amdgcn_make_buffer_rsrc((void*)x, 0, 512 * 1152, 0x00020000);
    const rsrc_t rk = __builtin_amdgcn_make_buffer_rsrc((void*)kc, 0, 256 * 8192, 0x00020000);
    const rsrc_t rv = __builtin_amdgcn_make_buffer_rsrc((void*)vc, 0, 256 * 8192, 0x00020000);
    const rsrc_t rq = __builtin_amdgcn_make_buffer_rsrc((void*)wq, 0, 576 * 8192, 0x00020000);
    const rsrc_t ro = __builtin_amdgcn_make_buffer_rsrc((void*)wo, 0, 4096 * 1152, 0x00020000);
    const u32 xr = 8 * g + (lane >> 3);
    const u32 vx = xr * 1152 + 16 * ((lane & 7) ^ ((xr >> 1) & 7));
    const u32 pr = 4 * g + row;
    const u32 pc = col ^ ((row << 2) | g);
    const u32 vq = pr * 8192 + 16 * pc;
    const u32 Lg = L + 1024 * g;
    const u32 r0 = 3 * ((bp + i) % 3);
    const u32 sx = bp * (64 * 1152), sq = 256 * h;
    auto step = [&](int s) {
      u32 ks = r0 + s; ks = ks >= 9 ? ks - 9 : ks;
      const u32 o = sx + ks * 128, o2 = sq + ks * (64 * 8192);
      dma<false>(Lg + (8 + (s & 3)) * PG, vx, rx, o); dma<false>(Lg + (8 + (s & 3)) * PG + 4096, vx, rx, o + 32 * 1152);
#pragma unroll
      for (int n = 0; n < 4; ++n) dma<true>(Lg + 2 * (s & 3) * PG + n * 4096, vq, rq, o2 + n * (16 * 8192));
    };
    step(0); step(1);
    {
      const rsrc_t rc = __builtin_amdgcn_make_buffer_rsrc((void*)(pc0 & ~255ull), 0, CODE_BYTES, 0x00020000);
      asm volatile("s_buffer_load_dword s100, %0, %1\n\ts_buffer_load_dword s101, %2, %3\n\ts_buffer_load_dword s101, %2, %3 offset:64\n\t"
                   "s_buffer_load_dword s101, %2, %3 offset:128\n\ts_buffer_load_dword s101, %4, %5"
                   :: "s"(rw), "s"(bp * (32 * SLAB) + EPOCH), "s"(rc), "s"(((b >> 3) * 4 + g) * 192), "s"(ry), "s"(bp * (64 * 1152)) : "s100", "s101", "memory");
    }
    {
      const u32 m0_ = 8 * i + 2 * g, c0_ = ((bp << 5) | m0_) * SLAB, own_ = b * SLAB + g * 8192;
      asm volatile("s_buffer_load_dword s101, %0, %1\n\ts_buffer_load_dword s101, %0, %1 offset:32768\n\t"
                   "s_buffer_load_dword s101, %0, %2\n\ts_buffer_load_dword s101, %0, %2 offset:32768\n\t"
                   "s_buffer_load_dword s101, %0, %3\n\ts_buffer_load_dword s101, %0, %3 offset:32768\n\t"
                   "s_buffer_load_dword s101, %0, %4\n\ts_buffer_load_dword s101, %0, %4 offset:4096"
                   :: "s"(rw), "s"(c0_ + IN9 + hq * 1024), "s"(c0_ + EPOCH + 256), "s"(c0_ + hq * 2048), "s"(own_ + 3968) : "s101", "memory");
    }
    u32 vkv = pr * 256 + 16 * pc, vo = pr * 1152 + 16 * pc, vo9 = (pr + 64 * (pc >> 3)) * 1152 + 16 * (pc & 7);
    u32 pl = L + 2 * PG + g * 4096 + row * 512 + col * 16;
    u32 vcl = row * 8;
    PIN(vkv); PIN(vo); PIN(vo9); PIN(pl); PIN(vcl);
    u32 bs = b;
    asm volatile("" : "+s"(bs));
    setup2();
    auto ldk = [&](u32 n) {
      const u32 o = ((2 * bp + n) * 16 + (h >> 1)) * 8192;
      dma<true>(Lg + (12 + n) * PG, vkv, rk, o); dma<true>(Lg + (12 + n) * PG + 4096, vkv, rk, o + 4096);
    };
    auto ldv = [&](u32 n) {
      const u32 o = ((2 * bp + (n & 1)) * 16 + 2 * hq + ((n >> 1) ^ (bp >> 2))) * 8192;
      dma<true>(Lg + (16 + n) * PG, vkv, rv, o); dma<true>(Lg + (16 + n) * PG + 4096, vkv, rv, o + 4096);
    };
    auto ldw1 = [&](int n, int pg, int k) {
      dma<true>(Lg + pg * PG + k * 4096, vo, ro, ((bs & 7) * 512 + 32 * (n ^ ((bs >> 5) << 1)) + 16 * k) * 1152 + 256 * ((bs >> 3) & 3));
    };
    auto ldw = [&](int n, int pg) { ldw1(n, pg, 0); ldw1(n, pg, 1); };
    auto ldn = [&](int n, int pg) {
      const u32 o = (h * 128 + 32 * n) * 1152 + 1024;
      dma<true>(Lg + pg * PG, vo9, ro, o); dma<true>(Lg + pg * PG + 4096, vo9, ro, o + 16 * 1152);
    };
    auto extra = [&](int t) {
      if (t == KPOS) { ldk(i & 1); ldk((i & 1) ^ 1); }
      const u32 grp = bp & 3;
      if ((t == SH0 && grp == 0) || (t == SH1 && grp == 1) || (t == SH2 && grp == 2) || (t == SH3 && grp == 3)) {
        ldv(i); ldw(0, 14); ldw(1, 15);
        dma<true>(Lg + (16 + (i ^ 1)) * PG, vo9, ro, (h * 128 + 16 * (bp & 3)) * 1152 + 1024);
      }
    };
    step(2);
    HV(0, 12); step(3); extra(0);
    HV(1, 12); step(4); extra(1);
    HV(2, 12); step(5); extra(2);
    HV(3, 12); step(6); extra(3);
    HV(4, 12); step(7); extra(4);
    HV(5, 12); step(8);
    HV(6, 12); ldv(i ^ 1); ldv(i ^ 2);
    HV(7, 10); ldv(i ^ 3);
    asm volatile("s_waitcnt lgkmcnt(0)\n\ts_mov_b32 %0, s100" : "=s"(tag) :: "memory");
    tag += 1;
    HV(8, 8);  ldw(2, 9);
    HV(9, 8);  WV(PACE); ldw(3, 10); WV(PACE); ldn(0, 2); WV(PACE); ldn(1, 3);
    HV(10, 8);
    rV(0, 0); rW(0, 14); SB(); rV(0, 1); rW(1, 14); SB(); rV(0, 2); rW(2, 15); SB(); rV(0, 3); rW(3, 15);
#pragma unroll
    for (int s = 0; s < 4; ++s) { u = MF32(vf[s], bw[s], s ? u : zero); rV(0, s + 4); }
    WV(PACE); ldw(4, 4); WV(PACE); ldw(5, 5); WV(PACE); ldw(6, 6); WV(PACE); ldw(7, 7);
    HV(11, 8);
    rW(4, 9); SB(); rW(5, 9); SB(); rW(6, 10); SB(); rW(7, 10); SB();
    ldw(8, 11);
#pragma unroll
    for (int s = 4; s < 8; ++s) u = MF32(vf[s], bw[s], u);
    WV(PACE); ldw(9, 0); WV(PACE); ldw(10, 1); WV(PACE); ldw(11, 8);
    __builtin_amdgcn_s_setprio(HPRIO);
    HV(12, 8);
    headW(1, 4, 5, 6, 7, [&](int s) { ldw1(12 + (s >> 1), 12 + (s >> 1), s & 1); });
    HV(13, 8);
    v4u p0, p1, p2, p3; v2u c0, c1, c2, c3;
    const u32 spl = ((bp << 5) | ((g ^ (bp >> 1)) << 3) | hq) * SLAB;
#define LOAD_P "buffer_load_dwordx4 %0, %8, %10, %11 offen sc1\n\tbuffer_load_dwordx4 %1, %8, %10, %11 offen offset:1024 sc1\n\t" \
               "buffer_load_dwordx4 %2, %8, %10, %11 offen offset:2048 sc1\n\tbuffer_load_dwordx4 %3, %8, %10, %11 offen offset:3072 sc1\n\t" \
               "buffer_load_dwordx2 %4, %9, %10, %12 offen sc1\n\tbuffer_load_dwordx2 %5, %9, %10, %12 offen offset:32 sc1\n\t" \
               "buffer_load_dwordx2 %6, %9, %10, %12 offen offset:64 sc1\n\tbuffer_load_dwordx2 %7, %9, %10, %12 offen offset:96 sc1"
#define P_ARGS "=&v"(p0), "=&v"(p1), "=&v"(p2), "=&v"(p3), "=&v"(c0), "=&v"(c1), "=&v"(c2), "=&v"(c3) : "v"(v16), "v"(vcl), "s"(rw), "s"(spl + POUT), "s"(spl + CP) : "memory"
    headW(2, 11, 0, 1, 8, [&](int s) { if (s == PPOS) asm volatile(LOAD_P : P_ARGS); });
    HV(14, 8);
    headW(3, 12, 13, 14, 15, nop);
    asm volatile("s_waitcnt vmcnt(0)" : "+v"(p0), "+v"(p1), "+v"(p2), "+v"(p3), "+v"(c0), "+v"(c1), "+v"(c2), "+v"(c3) :: "memory");
    STAMP(62);
    _Pragma("nounroll") for (int sp = 0; sp < SPIN_CAP; ++sp) {
      *(LAS v4u*)(size_t)pl = p0; *(LAS v4u*)(size_t)(pl + 256) = p1; *(LAS v4u*)(size_t)(pl + 2048) = p2; *(LAS v4u*)(size_t)(pl + 2304) = p3;
      const v2u f0 = fold(p1, fold(p0)), f1 = fold(p3, fold(p2));
      const v2u f = half8(row8(v2u{f0.x + f1.x, f0.y ^ f1.y}));
      if (__all(f.x + 4 * tag == (c0.x + c1.x) + (c2.x + c3.x) && f.y == (xor3(c0.y, c1.y, c2.y) ^ c3.y))) break;
      asm volatile("s_waitcnt lgkmcnt(0)\n\t" LOAD_P "\n\ts_waitcnt vmcnt(0)" : P_ARGS);
    }
    HT(15, 0);
  } else {
    setup2();
    const u32 mt = w >> 1;
    u32 aq[8][2], ax[2], ak[8], a9[4][2];
    const u32 xrow = 16 * w + col, key = 16 * (col >> 3) + (col & 7);
    ax[0] = L + 8 * PG + 128 * xrow + 16 * (row ^ ((xrow >> 1) & 7)); ax[1] = ax[0] ^ 64;
    PIN(ax[0]); PIN(ax[1]);
#pragma unroll
    for (int n = 0; n < 8; ++n) {
#pragma unroll
      for (int t = 0; t < 2; ++t) {
        aq[n][t] = L + 256 * (8 * row + 4 * t + q4) + 16 * ((2 * n + (p4 >> 1)) ^ ((q4 << 2) | ((2 * row + t) & 3))) + 8 * (p4 & 1);
        PIN(aq[n][t]);
      }
      ak[n] = L + (12 + mt) * PG + 256 * key + 16 * ((2 * n + (row >> 1)) ^ (((key & 3) << 2) | ((key >> 2) & 3))) + 8 * (row & 1);
      a9[n >> 1][n & 1] = L + (16 + mt + 2 * ((i >> 1) ^ (bp >> 2))) * PG + 256 * (16 * (row >> 1) + 4 * (row & 1) + 8 * (n & 1) + q4) +
                          16 * ((4 * (n >> 1) + p4) ^ ((q4 << 2) | ((row & 1) + 2 * (n & 1))));
      PIN(ak[n]); PIN(a9[n >> 1][n & 1]);
    }
    u32 v9 = (row << 3) * SLAB + col * 16, vc9 = (col & 7) == 0 ? (row << 3) * SLAB + (col >> 3) * SLAB : OOB, vcs = col == 0 ? row * 8 : OOB;
    PIN(v9); PIN(vc9); PIN(vcs);
    const u32 sp0 = b * SLAB + POUT + w * 1024, sp1 = b * SLAB + CP + w * 32;
    const u32 s9 = ((bp << 5) | (2 * w)) * SLAB;
    const u32 s9a = s9 + IN9 + me * 256, s9b = s9a + SLAB, s9c = s9 + C9 + me * 8;
    asm volatile("" :: "s"(sp0), "s"(sp1), "s"(s9a), "s"(s9b), "s"(s9c));

    const f32x4 z4 = {0.f, 0.f, 0.f, 0.f};
    f32x4 qa[8];
#pragma unroll
    for (int n = 0; n < 8; ++n) qa[n] = z4;
    bf16x8 A[16], B[2], kf[2][4];
    auto rA = [&](int sl, int n) { const u32 o = (2 * sl + (n >> 3)) * PG; A[n] = trd(aq[n & 7][0] + o, aq[n & 7][1] + o); };
    auto rB = [&](int sl, int k) { B[k] = rd16(ax[k] + sl * PG); };
    auto rK = [&](int T, int ks) {
      const s16x4 lo = *(LAS s16x4*)(size_t)(ak[2 * ks + T] + T * 2048), hi = *(LAS s16x4*)(size_t)(ak[2 * ks + 1 - T] + T * 2048);
      kf[T][ks] = __builtin_shufflevector(lo, hi, 0, 1, 2, 3, 4, 5, 6, 7);
    };
    bf16x8 Bc;
    auto head = [&](int sl, bool carry) {
      rB(sl, 0);
#pragma unroll
      for (int n = 0; n < 6; ++n) { rA(sl, n); if (carry) qa[n + 2] = MS16(A[n + 10], Bc, qa[n + 2]); else SB(); }
      rB(sl, 1);
    };
    auto p1 = [&](int t) {
      head(t & 3, t > 0);
#pragma unroll
      for (int n = 0; n < 10; ++n) { qa[n & 7] = MS16(A[n], B[n >> 3], qa[n & 7]); rA(t & 3, n + 6); }
      Bc = B[1];
      if (t == KPOS + 4) { rK(0, 0); rK(0, 1); rK(0, 2); rK(0, 3); }
      if (t == KPOS + 5) { rK(1, 0); rK(1, 1); rK(1, 2); rK(1, 3); }
    };
    CT(0); p1(0); CT(1); p1(1); CT(2); p1(2); CT(3); p1(3);
    asm volatile("s_buffer_load_dword %0, %1, %2\n\ts_waitcnt lgkmcnt(0)" : "=s"(tag) : "s"(rw), "s"(bp * (32 * SLAB) + EPOCH) : "memory");
    tag += 1;
    CT(4); p1(4); CT(5); p1(5); CB(6); p1(6); CB(7); p1(7);
    CB(8);
    head(0, true);
#pragma unroll
    for (int n = 0; n < 8; ++n) { qa[n] = MS16(A[n], B[0], qa[n]); if (n < 2) rA(0, n + 6); }
    CB(9);
    f32x4 sa = z4, sb = z4;
    {
      v4u q[4];
      auto pq = [&](int ks) { q[ks] = v4u{pk(qa[2 * ks][0], qa[2 * ks][1]), pk(qa[2 * ks][2], qa[2 * ks][3]), pk(qa[2 * ks + 1][0], qa[2 * ks + 1][1]), pk(qa[2 * ks + 1][2], qa[2 * ks + 1][3])}; PIN(q[ks]); };
#pragma unroll
      for (int n = 8; n < 14; ++n) { rA(0, n); SB(); }
      qa[0] = MS16(A[8], B[1], qa[0]); rA(0, 14);
      qa[1] = MS16(A[9], B[1], qa[1]); rA(0, 15);
      qa[2] = MS16(A[10], B[1], qa[2]);
      qa[3] = MS16(A[11], B[1], qa[3]);
      qa[4] = MS16(A[12], B[1], qa[4]);
      qa[5] = MS16(A[13], B[1], qa[5]);
      pq(0);
      qa[6] = MS16(A[14], B[1], qa[6]);
      qa[7] = MS16(A[15], B[1], qa[7]);
      pq(1);
      sa = MS16(kf[0][0], BC(q[0]), sa); sb = MS16(kf[1][0], BC(q[0]), sb);
      pq(2);
      sa = MS16(kf[0][1], BC(q[1]), sa); sb = MS16(kf[1][1], BC(q[1]), sb);
      pq(3);
      sa = MS16(kf[0][2], BC(q[2]), sa); sb = MS16(kf[1][2], BC(q[2]), sb);
      sa = MS16(kf[0][3], BC(q[3]), sa); sb = MS16(kf[1][3], BC(q[3]), sb);
    }
    CB(10);
    {
      bf16x8 vt[8], wt[16]; f32x4 ot[8], y[4]; v4u of[4];
      auto rT = [&](int n) { vt[n] = trd(a9[n >> 1][0] + 8 * (n & 1), a9[n >> 1][1] + 8 * (n & 1)); };
      auto rN = [&](int n) { const int ks = n >> 2, c = 4 * (ks >> 1) + (n & 3); wt[n] = trd(aq[c][0] + (2 + (ks & 1)) * PG, aq[c][1] + (2 + (ks & 1)) * PG); };
      rT(0); rT(1); rT(2); rT(3); SB();
      float mx, m2;
      asm volatile("s_nop 1\n\tv_max3_f32 %0, %2, %3, %4\n\tv_max3_f32 %1, %5, %6, %7\n\tv_max3_f32 %0, %0, %8, %9\n\tv_max_f32 %0, %0, %1\n\t"
                   "v_mov_b32 %1, %0\n\ts_nop 1\n\tv_permlane16_swap_b32 %0, %1\n\tv_max_f32 %0, %0, %1\n\t"
                   "v_mov_b32 %1, %0\n\ts_nop 1\n\tv_permlane32_swap_b32 %0, %1\n\tv_max_f32 %0, %0, %1"
                   : "=&v"(mx), "=&v"(m2) : "v"(sa[0]), "v"(sa[1]), "v"(sa[2]), "v"(sa[3]), "v"(sb[0]), "v"(sb[1]), "v"(sb[2]), "v"(sb[3]));
      const float c = 0.08838834764831845f * 1.4426950408889634f, mc = -mx * c;
      float e[8], z2;
#pragma unroll
      for (int n = 0; n < 4; ++n) { e[n] = __builtin_amdgcn_exp2f(__builtin_fmaf(sa[n], c, mc)); e[4 + n] = __builtin_amdgcn_exp2f(__builtin_fmaf(sb[n], c, mc)); }
      float z = ((e[0] + e[2]) + (e[4] + e[6])) + ((e[1] + e[3]) + (e[5] + e[7]));
      asm volatile("v_mov_b32 %1, %0\n\ts_nop 1\n\tv_permlane16_swap_b32 %0, %1\n\tv_add_f32 %0, %0, %1\n\t"
                   "v_mov_b32 %1, %0\n\ts_nop 1\n\tv_permlane32_swap_b32 %0, %1\n\tv_add_f32 %0, %0, %1" : "+v"(z), "=&v"(z2));
      const float r = __builtin_amdgcn_rcpf(z);
      const v4u pv = {pk(e[0] * r, e[1] * r), pk(e[2] * r, e[3] * r), pk(e[4] * r, e[5] * r), pk(e[6] * r, e[7] * r)};
      st16(pv, v16, rw, sp0);
      rT(4); rT(5); rT(6); rT(7);
      v2u ck;
#pragma unroll
      for (int n = 0; n < 8; ++n) {
        ot[n] = MS16(vt[n], BC(pv), z4);
        if (n == 0) { ck = row8(fold(pv)); PIN(ck); } else if (n == 1) { ck = half8(ck); ck.x += tag; st8(ck, vcs, rw, sp1); }
        else if (n >= 4) { rV(0, n - 4); rW(n - 4, 14 + ((n - 4) >> 1)); }
      }
      STAMP(61);
      CB(11);
#pragma unroll
      for (int s = 0; s < 4; ++s) {
        rN(2 * s); rN(2 * s + 1);
        of[s] = v4u{pk(ot[2 * s][0], ot[2 * s][1]), pk(ot[2 * s][2], ot[2 * s][3]), pk(ot[2 * s + 1][0], ot[2 * s + 1][1]), pk(ot[2 * s + 1][2], ot[2 * s + 1][3])};
        PIN(of[s]);
        u = MF32(vf[s], bw[s], s ? u : zero);
      }
#pragma unroll
      for (int n = 0; n < 16; ++n) {
        y[n & 3] = MS16(wt[n], BC(of[n >> 2]), n < 4 ? z4 : y[n & 3]);
        if (n < 8) rN(n + 8); else if (n & 1) rW(4 + ((n - 8) >> 1), 9 + ((n - 8) >> 2)); else rV(0, 4 + ((n - 8) >> 1));
      }
      v4u e0, e1; v2u g0, g1;
      CB(12);
      lagW(1, 4, 5, 6, 7, [&](int s) {
        if (s == 0) { e0 = v4u{pk(y[0][0], y[0][1]), pk(y[0][2], y[0][3]), pk(y[1][0], y[1][1]), pk(y[1][2], y[1][3])}; st16(e0, v9, rw, s9a); }
        else if (s == 1) { e1 = v4u{pk(y[2][0], y[2][1]), pk(y[2][2], y[2][3]), pk(y[3][0], y[3][1]), pk(y[3][2], y[3][3])}; st16(e1, v9, rw, s9b); }
        else if (s == 2) { g0 = fold(e0); PIN(g0); } else if (s == 3) { g1 = fold(e1); PIN(g1); }
        else if (s == 4) { g0 = row8(g0); g1 = row8(g1); g0 = col < 8 ? g0 : g1; PIN(g0); }
        else if (s == 5) { g0 = half8(g0); g0.x += tag; st8(g0, vc9, rw, s9c); } });
    }
    CB(13); lagW(2, 11, 0, 1, 8, nop);
    CB(14); lagW(3, 12, 13, 14, 15, nop);
#pragma unroll
    for (int s = 4; s < 8; ++s) u = MF32(vf[s], bw[s], u);
    CB(15);
  }
  {
    const u32 so = ((bp << 5) | (i << 3) | (bb * 4 + (ct >> 1))) * SLAB;
    const u32 sd0 = __builtin_amdgcn_readfirstlane(so + hq * 2048 + (ct & 1) * 1024), sd1 = sd0 + 2 * SLAB;
    const u32 sc0 = __builtin_amdgcn_readfirstlane(so + CMAIN + hq * 128 + (ct & 1) * 32);
    bf16x8 pf[8];
#pragma unroll
    for (int n = 0; n < 8; ++n) { pf[n] = rd16(ap + (n >> 1) * 4096 + (n & 1) * 1024); SB(); }
    uu[3][0] = pack8<0>(u); uu[3][1] = pack8<1>(u);
    f32x16 o = zero;
#pragma unroll
    for (int n = 0; n < 8; ++n) o = MF32(pf[n], BC(uu[n >> 1][n & 1]), o);
    const v4u d0 = pack8<0>(o); st16(d0, v16, rw, sd0);
    const v4u d1 = pack8<1>(o); st16(d1, v16, rw, sd1);
    const v2u f0 = row8(fold(d0)), f1 = row8(fold(d1));
    v2u f = half8(col < 8 ? f0 : f1); f.x += tag; st8(f, vcm, rw, sc0);
  }
  STAMP(58);
  if (w == 7) {
    const u32 vd = v16 + b * SLAB + IN9, vc = row * 8 + b * SLAB + C9;
    v4u d[8]; v2u c[8];
    _Pragma("nounroll") for (int sp = 0; sp < SPIN_CAP; ++sp) {
      asm volatile("buffer_load_dwordx4 %0, %16, %18, 0 offen sc1\n\tbuffer_load_dwordx4 %1, %16, %18, 0 offen offset:1024 sc1\n\t"
                   "buffer_load_dwordx4 %2, %16, %18, 0 offen offset:2048 sc1\n\tbuffer_load_dwordx4 %3, %16, %18, 0 offen offset:3072 sc1\n\t"
                   "buffer_load_dwordx4 %4, %16, %18, %19 offen sc1\n\tbuffer_load_dwordx4 %5, %16, %18, %19 offen offset:1024 sc1\n\t"
                   "buffer_load_dwordx4 %6, %16, %18, %19 offen offset:2048 sc1\n\tbuffer_load_dwordx4 %7, %16, %18, %19 offen offset:3072 sc1\n\t"
                   "buffer_load_dwordx2 %8, %17, %18, 0 offen sc1\n\tbuffer_load_dwordx2 %9, %17, %18, 0 offen offset:32 sc1\n\t"
                   "buffer_load_dwordx2 %10, %17, %18, 0 offen offset:64 sc1\n\tbuffer_load_dwordx2 %11, %17, %18, 0 offen offset:96 sc1\n\t"
                   "buffer_load_dwordx2 %12, %17, %18, 0 offen offset:128 sc1\n\tbuffer_load_dwordx2 %13, %17, %18, 0 offen offset:160 sc1\n\t"
                   "buffer_load_dwordx2 %14, %17, %18, 0 offen offset:192 sc1\n\tbuffer_load_dwordx2 %15, %17, %18, 0 offen offset:224 sc1\n\t"
                   "s_waitcnt vmcnt(0)"
                   : "=&v"(d[0]), "=&v"(d[1]), "=&v"(d[2]), "=&v"(d[3]), "=&v"(d[4]), "=&v"(d[5]), "=&v"(d[6]), "=&v"(d[7]),
                     "=&v"(c[0]), "=&v"(c[1]), "=&v"(c[2]), "=&v"(c[3]), "=&v"(c[4]), "=&v"(c[5]), "=&v"(c[6]), "=&v"(c[7])
                   : "v"(vd), "v"(vc), "s"(rw), "s"(4096) : "memory");
      const v2u f0 = fold(d[3], fold(d[2], fold(d[1], fold(d[0])))), f1 = fold(d[7], fold(d[6], fold(d[5], fold(d[4]))));
      const v2u f = half8(row8(v2u{f0.x + f1.x, f0.y ^ f1.y}));
      u32 cs = 0, cx = 0;
#pragma unroll
      for (int n = 0; n < 8; ++n) { cs += c[n].x; cx ^= c[n].y; }
      if (__all(f.x + 8 * tag == cs && f.y == cx)) break;
      __builtin_amdgcn_s_sleep(2);
    }
    f32x4 y = {0.f, 0.f, 0.f, 0.f};
#pragma unroll
    for (int n = 0; n < 8; ++n) y = MF16(BC(sel), BC(d[n]), y);
    __builtin_amdgcn_raw_buffer_store_b64(v2u{pk(y[0], y[1]), pk(y[2], y[3])}, ry,
                                          row < 2 ? (64 * bp + 16 * (hq >> 1) + col) * 1152 + row * 32 + (512 + 32 * (hq & 1) + 4 * i) * 2 : OOB, 0, 0);
    if (me == 0) __builtin_amdgcn_raw_buffer_store_b32(tag, rw, lane == 0 ? bp * (32 * SLAB) + EPOCH : OOB, 0, 16);
  } else {
    asm volatile("s_waitcnt vmcnt(0)" ::: "memory");
  }
  STAMP(59);
  {
    v4u ga, gb; v2u ca, cb;
    _Pragma("nounroll") for (int sp = 0; sp < SPIN_CAP; ++sp) {
      asm volatile("buffer_load_dwordx4 %0, %4, %7, 0 offen sc1\n\tbuffer_load_dwordx4 %1, %4, %7, %5 offen sc1\n\t"
                   "buffer_load_dwordx2 %2, %6, %7, 0 offen sc1\n\tbuffer_load_dwordx2 %3, %6, %7, 0 offen offset:512 sc1\n\ts_waitcnt vmcnt(0)"
                   : "=&v"(ga), "=&v"(gb), "=&v"(ca), "=&v"(cb) : "v"(vg), "s"(8192), "v"(vgc), "s"(rw) : "memory");
      const v2u f = half8(row8(fold(gb, fold(ga))));
      if (__all(f.x + 2 * tag == ca.x + cb.x && f.y == (ca.y ^ cb.y))) break;
      __builtin_amdgcn_s_sleep(1);
    }
    f32x4 y = {0.f, 0.f, 0.f, 0.f};
    y = MF16(BC(ga), BC(sel), y); y = MF16(BC(gb), BC(sel), y);
    __builtin_amdgcn_raw_buffer_store_b64(v2u{pk(y[0], y[1]), pk(y[2], y[3])}, ry, vy,
                                          4 * ((w >> 1) & 1) * 1152 + (128 * i + 64 * (hq & 1) + 32 * (w >> 2) + 16 * (w & 1)) * 2, 0);
  }
  STAMP(60);
}

extern "C" void kernel_launch(void* const* d_in, const int* in_sizes, int n_in, void* d_out, int out_size, void* d_ws, size_t ws_size,
                              hipStream_t stream) {
  gqa_fused<<<256, 512, 0, stream>>>(d_in[0], d_in[1], d_in[2], d_in[3], d_in[4], d_out, (char*)d_ws
  );
}
```

```cpp
#define DIAGA 30
#define DIAGB 36
#include <hip/hip_runtime.h>
#include <hip/hip_bf16.h>

typedef unsigned long long u64;
typedef unsigned u32;
typedef u32 v4u __attribute__((ext_vector_type(4)));
typedef u32 v2u __attribute__((ext_vector_type(2)));
typedef short s16x4 __attribute__((ext_vector_type(4)));
typedef short bf16x8 __attribute__((ext_vector_type(8)));
typedef float f32x16 __attribute__((ext_vector_type(16)));
typedef float f32x4 __attribute__((ext_vector_type(4)));
typedef float f32x2 __attribute__((ext_vector_type(2)));
typedef __bf16 b16x2 __attribute__((ext_vector_type(2)));
#define LAS __attribute__((address_space(3)))
#define GAS __attribute__((address_space(1)))
#define DI __device__ __forceinline__
typedef __amdgpu_buffer_rsrc_t rsrc_t;

constexpr u32 PG = 8192;
constexpr u32 SLAB = 32768;
constexpr u32 IN9 = 16384, POUT = 24576, CMAIN = 28672, C9 = 29696, CP = 30208, EPOCH = 31744;
constexpr u32 WS_BYTES = 256 * SLAB;
constexpr u32 OOB = 0x80000000u;
constexpr int SPIN_CAP = 1 << 16;
#define CODE_BYTES 17920
#define PACE 6
#define PSLEEP 1
#define KPOS 0
#define SH0 1
#define SH1 2
#define SH2 3
#define SH3 4
#define HPRIO 2
#define PPOS 3
#define GSLEEP 20

#define RD2(t, c) asm volatile("s_memrealtime %0\n\ts_memtime %1\n\ts_waitcnt lgkmcnt(0)" : "=s"(t), "=s"(c) :: "memory")
#define STAMP(i) do { if ((i) == 0) { RD2(dg_t0, dg_ca); dg_ta = dg_tb = dg_t0; dg_cb = dg_ca; } else if ((i) == DIAGA) { RD2(dg_ta, dg_ca); } else if ((i) == DIAGB) { RD2(dg_tb, dg_cb); } } while (0)
#define SB() __builtin_amdgcn_sched_barrier(0)
#define HT(k, N) asm volatile("s_waitcnt vmcnt(%0) lgkmcnt(0)\n\ts_barrier" :: "i"(N) : "memory")
#define CT(k) do { SB(); asm volatile("s_waitcnt lgkmcnt(0)\n\ts_barrier" ::: "memory"); STAMP(3 * (k) + 3); SB(); } while (0)
#define CB(k) do { SB(); asm volatile("s_barrier" ::: "memory"); STAMP(3 * (k) + 3); SB(); } while (0)
#define HV(k, N) asm volatile("s_waitcnt vmcnt(%0)\n\ts_barrier" :: "i"(N) : "memory")
#define WV(N) asm volatile("s_waitcnt vmcnt(%0)" :: "i"(N) : "memory")
#define PIN(v) asm volatile("" : "+v"(v))

template <bool NT> DI void dma(u32 lds, u32 voff, rsrc_t rs, u32 soff) {
  if constexpr (NT) asm volatile("s_mov_b32 m0, %0\n\ts_nop 0\n\tbuffer_load_dwordx4 %1, %2, %3 offen nt lds" :: "s"(lds), "v"(voff), "s"(rs), "s"(soff) : "memory");
  else              asm volatile("s_mov_b32 m0, %0\n\ts_nop 0\n\tbuffer_load_dwordx4 %1, %2, %3 offen lds" :: "s"(lds), "v"(voff), "s"(rs), "s"(soff) : "memory");
}
DI void st16(v4u v, u32 voff, rsrc_t rs, u32 soff) {
  asm volatile("buffer_store_dwordx4 %0, %1, %2, %3 offen sc1\n\ts_nop 1" :: "v"(v), "v"(voff), "s"(rs), "s"(soff) : "memory");
}
DI void st8(v2u v, u32 voff, rsrc_t rs, u32 soff) {
  asm volatile("buffer_store_dwordx2 %0, %1, %2, %3 offen sc1" :: "v"(v), "v"(voff), "s"(rs), "s"(soff) : "memory");
}
DI s16x4 tr4(u32 a) { return __builtin_amdgcn_ds_read_tr16_b64_v4i16((LAS s16x4*)(size_t)a); }
DI bf16x8 trd(u32 a0, u32 a1) { return __builtin_shufflevector(tr4(a0), tr4(a1), 0, 1, 2, 3, 4, 5, 6, 7); }
DI bf16x8 rd16(u32 a) { return *(LAS bf16x8*)(size_t)a; }
DI u32 pk(float a, float b) { f32x2 v = {a, b}; return __builtin_bit_cast(u32, __builtin_convertvector(v, b16x2)); }
template <int S> DI v4u pack8(const f32x16& c) {
  v4u r = {pk(c[8 * S], c[8 * S + 1]), pk(c[8 * S + 2], c[8 * S + 3]), pk(c[8 * S + 4], c[8 * S + 5]), pk(c[8 * S + 6], c[8 * S + 7])};
  PIN(r);
  return r;
}
DI f32x16 MF32(bf16x8 a, bf16x8 b, f32x16 c) { SB(); f32x16 d = __builtin_amdgcn_mfma_f32_32x32x16_bf16(a, b, c, 0, 0, 0); PIN(d); SB(); return d; }
DI f32x4 MS16(bf16x8 a, bf16x8 b, f32x4 c) { SB(); f32x4 d = __builtin_amdgcn_mfma_f32_16x16x32_bf16(a, b, c, 0, 0, 0); PIN(d); SB(); return d; }
#define MF16(a, b, c) __builtin_amdgcn_mfma_f32_16x16x32_bf16((a), (b), (c), 0, 0, 0)
#define BC(v) __builtin_bit_cast(bf16x8, (v))
template <int C> DI float dppf(float v) { return __builtin_bit_cast(float, __builtin_amdgcn_mov_dpp(__builtin_bit_cast(int, v), C, 0xF, 0xF, true)); }
template <int C> DI u32 dppu(u32 v) { return (u32)__builtin_amdgcn_mov_dpp((int)v, C, 0xF, 0xF, true); }
DI u32 xor3(u32 a, u32 b, u32 c) { return __builtin_amdgcn_bitop3_b32(a, b, c, 0x96); }
DI v2u fold(v4u d) { return v2u{(d.x + d.y + d.z) + d.w, xor3(d.x, d.y, d.z) ^ d.w}; }
DI v2u fold(v4u d, v2u a) { return v2u{(d.x + d.y + a.x) + d.z + d.w, xor3(xor3(d.x, d.y, a.y), d.z, d.w)}; }
DI v2u row8(v2u v) { return v2u{v.x + dppu<0x128>(v.x), v.y ^ dppu<0x128>(v.y)}; }
DI v2u half8(v2u v) {
  v.x += dppu<0x141>(v.x); v.y ^= dppu<0x141>(v.y); v.x += dppu<0x4E>(v.x); v.y ^= dppu<0x4E>(v.y);
  v.x += dppu<0xB1>(v.x); v.y ^= dppu<0xB1>(v.y);
  return v;
}

__global__ void __launch_bounds__(512, 2)
gqa_fused(const void* __restrict__ x, const void* __restrict__ kc, const void* __restrict__ vc, const void* __restrict__ wq,
          const void* __restrict__ wo, void* __restrict__ out, char* __restrict__ ws
) {
  __shared__ __attribute__((aligned(1024))) char smem[20 * PG];
  u64 pc0;
  asm volatile("s_getpc_b64 %0" : "=s"(pc0));
  const u32 L = (u32)(size_t)(LAS char*)smem;
  const u32 tid = threadIdx.x, lane = tid & 63;
  const u32 w = __builtin_amdgcn_readfirstlane(tid >> 6);
  const u32 b = blockIdx.x, hq = b & 7, bp = b >> 5, i = (b >> 3) & 3, h = 4 * hq + i;
  u64 dg_t0, dg_ta, dg_tb, dg_ca, dg_cb;
  STAMP(0);
  const rsrc_t rw = __builtin_amdgcn_make_buffer_rsrc((void*)ws, 0, WS_BYTES, 0x00020000);
  const rsrc_t ry = __builtin_amdgcn_make_buffer_rsrc(out, 0, 512 * 1152, 0x00020000);
  const u32 me = hq * 4 + i, row = lane >> 4, col = lane & 15;
  const u32 hh = lane >> 5, l31 = lane & 31;
  const u32 ct = w & 3, bb = w >> 2, q4 = col >> 2, p4 = lane & 3;
  f32x16 zero;
#pragma unroll
  for (int n = 0; n < 16; ++n) zero[n] = 0.f;
  u32 tag, vy, vg, vgc, v16, vcm, ap, aw[2][2], av[8]; v4u sel;
  bf16x8 vf[8], bw[8]; v4u uu[4][2]; f32x16 u;
  auto setup2 = [&]() {
    const u32 one = (col & 1) ? 0x3F800000u : 0x00003F80u;
    sel = v4u{(col >> 1) == 0 ? one : 0u, (col >> 1) == 1 ? one : 0u, (col >> 1) == 2 ? one : 0u, (col >> 1) == 3 ? one : 0u};
    const u32 yrow = 64 * bp + 32 * (hq >> 2) + 16 * ((hq >> 1) & 1) + (col & 3) + 8 * ((col >> 2) & 1);
    vy = col < 8 ? yrow * 1152 + 8 * row : OOB;
    vg = row * 2048 + w * 256 + col * 16 + b * SLAB; vgc = row * 128 + w * 8 + b * SLAB + CMAIN;
    v16 = lane * 16; vcm = (col & 7) == 0 ? (col >> 3) * (2 * SLAB) + row * 8 : OOB;
    ap = L + 2 * PG + bb * 2048 + lane * 16;
    PIN(sel); PIN(vy); PIN(vg); PIN(vgc); PIN(v16); PIN(vcm); PIN(ap);
#pragma unroll
    for (int t = 0; t < 2; ++t) {
      aw[0][t] = L + 256 * (8 * hh + 4 * t + q4) + 16 * ((4 * ct + 2 * (row & 1) + (p4 >> 1)) ^ ((q4 << 2) | (2 * hh + t))) + 8 * (p4 & 1);
      aw[1][t] = aw[0][t] + 8 * PG;
      PIN(aw[0][t]); PIN(aw[1][t]);
    }
    const u32 arow = 256 * l31 + 16 * (hh ^ (((l31 & 3) << 2) | ((l31 >> 2) & 3)));
#pragma unroll
    for (int n = 0; n < 8; ++n) { av[n] = L + (16 + bb) * PG + (arow ^ (128 * (bp & 1)) ^ (32 * n)); PIN(av[n]); }
  };
  auto rV = [&](int slot, int s) { vf[s] = rd16(av[s] + 2 * slot * PG); };
  auto rW = [&](int s, int pg) { const u32 o = (pg & 7) * PG + (s & 1) * 4096; bw[s] = trd(aw[pg >> 3][0] + o, aw[pg >> 3][1] + o); };
  auto headW = [&](int hs, int p0, int p1, int p2, int p3, auto f) {
    const int pg[4] = {p0, p1, p2, p3};
    const f32x16 o = u;
    rW(0, p0); SB(); rW(1, p0); SB(); rW(2, p1); SB(); rW(3, p1); SB();
    uu[hs - 1][0] = pack8<0>(o); uu[hs - 1][1] = pack8<1>(o);
#pragma unroll
    for (int s = 0; s < 8; ++s) {
      u = MF32(vf[s], bw[s], s ? u : zero);
      if (s < 4) rW(s + 4, pg[2 + (s >> 1)]);
      if (hs == 1) rV(1, s);
      f(s);
    }
  };
  auto lagW = [&](int hs, int p0, int p1, int p2, int p3, auto f) {
    const int pg[4] = {p0, p1, p2, p3};
#pragma unroll
    for (int s = 4; s < 8; ++s) { u = MF32(vf[s], bw[s], u); rW(s - 4, pg[(s - 4) >> 1]); if (hs == 2) rV(1, s); f(s - 4); }
    const f32x16 o = u;
#pragma unroll
    for (int s = 0; s < 4; ++s) {
      u = MF32(vf[s], bw[s], s ? u : zero);
      rW(s + 4, pg[2 + (s >> 1)]);
      if (hs == 1) rV(1, s);
      if (s == 2) uu[hs - 1][0] = pack8<0>(o);
      if (s == 3) uu[hs - 1][1] = pack8<1>(o);
      f(s + 4);
    }
  };
  auto nop = [](int) {};
  asm volatile("" :: "s"(x), "s"(kc), "s"(vc), "s"(wq), "s"(wo), "s"(out), "s"(ws));

  if (__builtin_expect(w >= 4, 1)) {
    const u32 g = w - 4;
    const rsrc_t rx = __builtin_amdgcn_make_buffer_rsrc((void*)x, 0, 512 * 1152, 0x00020000);
    const rsrc_t rk = __builtin_amdgcn_make_buffer_rsrc((void*)kc, 0, 256 * 8192, 0x00020000);
    const rsrc_t rv = __builtin_amdgcn_make_buffer_rsrc((void*)vc, 0, 256 * 8192, 0x00020000);
    const rsrc_t rq = __builtin_amdgcn_make_buffer_rsrc((void*)wq, 0, 576 * 8192, 0x00020000);
    const rsrc_t ro = __builtin_amdgcn_make_buffer_rsrc((void*)wo, 0, 4096 * 1152, 0x00020000);
    const u32 xr = 8 * g + (lane >> 3);
    const u32 vx = xr * 1152 + 16 * ((lane & 7) ^ ((xr >> 1) & 7));
    const u32 pr = 4 * g + row;
    const u32 pc = col ^ ((row << 2) | g);
    const u32 vq = pr * 8192 + 16 * pc;
    const u32 Lg = L + 1024 * g;
    const u32 r0 = 3 * ((bp + i) % 3);
    const u32 sx = bp * (64 * 1152), sq = 256 * h;
    auto step = [&](int s) {
      u32 ks = r0 + s; ks = ks >= 9 ? ks - 9 : ks;
      const u32 o = sx + ks * 128, o2 = sq + ks * (64 * 8192);
      dma<false>(Lg + (8 + (s & 3)) * PG, vx, rx, o); dma<false>(Lg + (8 + (s & 3)) * PG + 4096, vx, rx, o + 32 * 1152);
#pragma unroll
      for (int n = 0; n < 4; ++n) dma<true>(Lg + 2 * (s & 3) * PG + n * 4096, vq, rq, o2 + n * (16 * 8192));
    };
    step(0); step(1);
    {
      const rsrc_t rc = __builtin_amdgcn_make_buffer_rsrc((void*)(pc0 & ~255ull), 0, CODE_BYTES, 0x00020000);
      asm volatile("s_buffer_load_dword s100, %0, %1\n\ts_buffer_load_dword s101, %2, %3\n\ts_buffer_load_dword s101, %2, %3 offset:64\n\t"
                   "s_buffer_load_dword s101, %2, %3 offset:128\n\ts_buffer_load_dword s101, %4, %5"
                   :: "s"(rw), "s"(bp * (32 * SLAB) + EPOCH), "s"(rc), "s"(((b >> 3) * 4 + g) * 192), "s"(ry), "s"(bp * (64 * 1152)) : "s100", "s101", "memory");
    }
    u32 vkv = pr * 256 + 16 * pc, vo = pr * 1152 + 16 * pc, vo9 = (pr + 64 * (pc >> 3)) * 1152 + 16 * (pc & 7);
    u32 pl = L + 2 * PG + g * 4096 + row * 512 + col * 16;
    u32 vcl = row * 8;
    PIN(vkv); PIN(vo); PIN(vo9); PIN(pl); PIN(vcl);
    u32 bs = b;
    asm volatile("" : "+s"(bs));
    setup2();
    auto ldk = [&](u32 n) {
      const u32 o = ((2 * bp + n) * 16 + (h >> 1)) * 8192;
      dma<true>(Lg + (12 + n) * PG, vkv, rk, o); dma<true>(Lg + (12 + n) * PG + 4096, vkv, rk, o + 4096);
    };
    auto ldv = [&](u32 n) {
      const u32 o = ((2 * bp + (n & 1)) * 16 + 2 * hq + ((n >> 1) ^ (bp >> 2))) * 8192;
      dma<true>(Lg + (16 + n) * PG, vkv, rv, o); dma<true>(Lg + (16 + n) * PG + 4096, vkv, rv, o + 4096);
    };
    auto ldw1 = [&](int n, int pg, int k) {
      dma<true>(Lg + pg * PG + k * 4096, vo, ro, ((bs & 7) * 512 + 32 * (n ^ ((bs >> 5) << 1)) + 16 * k) * 1152 + 256 * ((bs >> 3) & 3));
    };
    auto ldw = [&](int n, int pg) { ldw1(n, pg, 0); ldw1(n, pg, 1); };
    auto ldn = [&](int n, int pg) {
      const u32 o = (h * 128 + 32 * n) * 1152 + 1024;
      dma<true>(Lg + pg * PG, vo9, ro, o); dma<true>(Lg + pg * PG + 4096, vo9, ro, o + 16 * 1152);
    };
    auto extra = [&](int t) {
      if (t == KPOS) { ldk(i & 1); ldk((i & 1) ^ 1); }
      const u32 grp = bp & 3;
      if ((t == SH0 && grp == 0) || (t == SH1 && grp == 1) || (t == SH2 && grp == 2) || (t == SH3 && grp == 3)) {
        ldv(i); ldw(0, 14); ldw(1, 15);
        dma<true>(Lg + (16 + (i ^ 1)) * PG, vo9, ro, (h * 128 + 16 * (bp & 3)) * 1152 + 1024);
      }
    };
    step(2);
    HV(0, 12); step(3); extra(0);
    HV(1, 12); step(4); extra(1);
    HV(2, 12); step(5); extra(2);
    HV(3, 12); step(6); extra(3);
    HV(4, 12); step(7); extra(4);
    HV(5, 12); step(8);
    HV(6, 12); ldv(i ^ 1); ldv(i ^ 2);
    HV(7, 10); ldv(i ^ 3);
    asm volatile("s_waitcnt lgkmcnt(0)\n\ts_mov_b32 %0, s100" : "=s"(tag) :: "memory");
    tag += 1;
    HV(8, 8);  ldw(2, 9);
    HV(9, 8);  WV(PACE); ldw(3, 10); WV(PACE); ldn(0, 2); WV(PACE); ldn(1, 3);
    HV(10, 8);
    rV(0, 0); rW(0, 14); SB(); rV(0, 1); rW(1, 14); SB(); rV(0, 2); rW(2, 15); SB(); rV(0, 3); rW(3, 15);
#pragma unroll
    for (int s = 0; s < 4; ++s) { u = MF32(vf[s], bw[s], s ? u : zero); rV(0, s + 4); }
    WV(PACE); ldw(4, 4); WV(PACE); ldw(5, 5); WV(PACE); ldw(6, 6); WV(PACE); ldw(7, 7);
    HV(11, 8);
    rW(4, 9); SB(); rW(5, 9); SB(); rW(6, 10); SB(); rW(7, 10); SB();
    ldw(8, 11);
#pragma unroll
    for (int s = 4; s < 8; ++s) u = MF32(vf[s], bw[s], u);
    WV(PACE); ldw(9, 0); WV(PACE); ldw(10, 1); WV(PACE); ldw(11, 8);
    __builtin_amdgcn_s_setprio(HPRIO);
    HV(12, 8);
    headW(1, 4, 5, 6, 7, [&](int s) { ldw1(12 + (s >> 1), 12 + (s >> 1), s & 1); });
    HV(13, 8);
    v4u p0, p1, p2, p3; v2u c0, c1, c2, c3;
    const u32 spl = ((bp << 5) | ((g ^ (bp >> 1)) << 3) | hq) * SLAB;
#define LOAD_P "buffer_load_dwordx4 %0, %8, %10, %11 offen sc1\n\tbuffer_load_dwordx4 %1, %8, %10, %11 offen offset:1024 sc1\n\t" \
               "buffer_load_dwordx4 %2, %8, %10, %11 offen offset:2048 sc1\n\tbuffer_load_dwordx4 %3, %8, %10, %11 offen offset:3072 sc1\n\t" \
               "buffer_load_dwordx2 %4, %9, %10, %12 offen sc1\n\tbuffer_load_dwordx2 %5, %9, %10, %12 offen offset:32 sc1\n\t" \
               "buffer_load_dwordx2 %6, %9, %10, %12 offen offset:64 sc1\n\tbuffer_load_dwordx2 %7, %9, %10, %12 offen offset:96 sc1"
#define P_ARGS "=&v"(p0), "=&v"(p1), "=&v"(p2), "=&v"(p3), "=&v"(c0), "=&v"(c1), "=&v"(c2), "=&v"(c3) : "v"(v16), "v"(vcl), "s"(rw), "s"(spl + POUT), "s"(spl + CP) : "memory"
    headW(2, 11, 0, 1, 8, [&](int s) { if (s == PPOS) asm volatile(LOAD_P : P_ARGS); });
    HV(14, 8);
    headW(3, 12, 13, 14, 15, nop);
    asm volatile("s_waitcnt vmcnt(0)" : "+v"(p0), "+v"(p1), "+v"(p2), "+v"(p3), "+v"(c0), "+v"(c1), "+v"(c2), "+v"(c3) :: "memory");
    STAMP(62);
    _Pragma("nounroll") for (int sp = 0; sp < SPIN_CAP; ++sp) {
      *(LAS v4u*)(size_t)pl = p0; *(LAS v4u*)(size_t)(pl + 256) = p1; *(LAS v4u*)(size_t)(pl + 2048) = p2; *(LAS v4u*)(size_t)(pl + 2304) = p3;
      const v2u f0 = fold(p1, fold(p0)), f1 = fold(p3, fold(p2));
      const v2u f = half8(row8(v2u{f0.x + f1.x, f0.y ^ f1.y}));
      if (__all(f.x + 4 * tag == (c0.x + c1.x) + (c2.x + c3.x) && f.y == (xor3(c0.y, c1.y, c2.y) ^ c3.y))) break;
      asm volatile("s_waitcnt lgkmcnt(0)\n\t" LOAD_P "\n\ts_waitcnt vmcnt(0)" : P_ARGS);
    }
    HT(15, 0);
  } else {
    setup2();
    const u32 mt = w >> 1;
    u32 aq[8][2], ax[2], ak[8], a9[4][2];
    const u32 xrow = 16 * w + col, key = 16 * (col >> 3) + (col & 7);
    ax[0] = L + 8 * PG + 128 * xrow + 16 * (row ^ ((xrow >> 1) & 7)); ax[1] = ax[0] ^ 64;
    PIN(ax[0]); PIN(ax[1]);
#pragma unroll
    for (int n = 0; n < 8; ++n) {
#pragma unroll
      for (int t = 0; t < 2; ++t) {
        aq[n][t] = L + 256 * (8 * row + 4 * t + q4) + 16 * ((2 * n + (p4 >> 1)) ^ ((q4 << 2) | ((2 * row + t) & 3))) + 8 * (p4 & 1);
        PIN(aq[n][t]);
      }
      ak[n] = L + (12 + mt) * PG + 256 * key + 16 * ((2 * n + (row >> 1)) ^ (((key & 3) << 2) | ((key >> 2) & 3))) + 8 * (row & 1);
      a9[n >> 1][n & 1] = L + (16 + mt + 2 * ((i >> 1) ^ (bp >> 2))) * PG + 256 * (16 * (row >> 1) + 4 * (row & 1) + 8 * (n & 1) + q4) +
                          16 * ((4 * (n >> 1) + p4) ^ ((q4 << 2) | ((row & 1) + 2 * (n & 1))));
      PIN(ak[n]); PIN(a9[n >> 1][n & 1]);
    }
    u32 v9 = (row << 3) * SLAB + col * 16, vc9 = (col & 7) == 0 ? (row << 3) * SLAB + (col >> 3) * SLAB : OOB, vcs = col == 0 ? row * 8 : OOB;
    PIN(v9); PIN(vc9); PIN(vcs);
    const u32 sp0 = b * SLAB + POUT + w * 1024, sp1 = b * SLAB + CP + w * 32;
    const u32 s9 = ((bp << 5) | (2 * w)) * SLAB;
    const u32 s9a = s9 + IN9 + me * 256, s9b = s9a + SLAB, s9c = s9 + C9 + me * 8;
    asm volatile("" :: "s"(sp0), "s"(sp1), "s"(s9a), "s"(s9b), "s"(s9c));

    const f32x4 z4 = {0.f, 0.f, 0.f, 0.f};
    f32x4 qa[8];
#pragma unroll
    for (int n = 0; n < 8; ++n) qa[n] = z4;
    bf16x8 A[16], B[2], kf[2][4];
    auto rA = [&](int sl, int n) { const u32 o = (2 * sl + (n >> 3)) * PG; A[n] = trd(aq[n & 7][0] + o, aq[n & 7][1] + o); };
    auto rB = [&](int sl, int k) { B[k] = rd16(ax[k] + sl * PG); };
    auto rK = [&](int T, int ks) {
      const s16x4 lo = *(LAS s16x4*)(size_t)(ak[2 * ks + T] + T * 2048), hi = *(LAS s16x4*)(size_t)(ak[2 * ks + 1 - T] + T * 2048);
      kf[T][ks] = __builtin_shufflevector(lo, hi, 0, 1, 2, 3, 4, 5, 6, 7);
    };
    bf16x8 Bc;
    auto head = [&](int sl, bool carry) {
      rB(sl, 0);
#pragma unroll
      for (int n = 0; n < 6; ++n) { rA(sl, n); if (carry) qa[n + 2] = MS16(A[n + 10], Bc, qa[n + 2]); else SB(); }
      rB(sl, 1);
    };
    auto p1 = [&](int t) {
      head(t & 3, t > 0);
#pragma unroll
      for (int n = 0; n < 10; ++n) { qa[n & 7] = MS16(A[n], B[n >> 3], qa[n & 7]); rA(t & 3, n + 6); }
      Bc = B[1];
      if (t == KPOS + 4) { rK(0, 0); rK(0, 1); rK(0, 2); rK(0, 3); }
      if (t == KPOS + 5) { rK(1, 0); rK(1, 1); rK(1, 2); rK(1, 3); }
    };
    CT(0); p1(0); CT(1); p1(1); CT(2); p1(2); CT(3); p1(3);
    asm volatile("s_buffer_load_dword %0, %1, %2\n\ts_waitcnt lgkmcnt(0)" : "=s"(tag) : "s"(rw), "s"(bp * (32 * SLAB) + EPOCH) : "memory");
    tag += 1;
    CT(4); p1(4); CT(5); p1(5); CB(6); p1(6); CB(7); p1(7);
    CB(8);
    head(0, true);
#pragma unroll
    for (int n = 0; n < 8; ++n) { qa[n] = MS16(A[n], B[0], qa[n]); if (n < 2) rA(0, n + 6); }
    CB(9);
    f32x4 sa = z4, sb = z4;
    {
      v4u q[4];
      auto pq = [&](int ks) { q[ks] = v4u{pk(qa[2 * ks][0], qa[2 * ks][1]), pk(qa[2 * ks][2], qa[2 * ks][3]), pk(qa[2 * ks + 1][0], qa[2 * ks + 1][1]), pk(qa[2 * ks + 1][2], qa[2 * ks + 1][3])}; PIN(q[ks]); };
#pragma unroll
      for (int n = 8; n < 14; ++n) { rA(0, n); SB(); }
      qa[0] = MS16(A[8], B[1], qa[0]); rA(0, 14);
      qa[1] = MS16(A[9], B[1], qa[1]); rA(0, 15);
      qa[2] = MS16(A[10], B[1], qa[2]);
      qa[3] = MS16(A[11], B[1], qa[3]);
      qa[4] = MS16(A[12], B[1], qa[4]);
      qa[5] = MS16(A[13], B[1], qa[5]);
      pq(0);
      qa[6] = MS16(A[14], B[1], qa[6]);
      qa[7] = MS16(A[15], B[1], qa[7]);
      pq(1);
      sa = MS16(kf[0][0], BC(q[0]), sa); sb = MS16(kf[1][0], BC(q[0]), sb);
      pq(2);
      sa = MS16(kf[0][1], BC(q[1]), sa); sb = MS16(kf[1][1], BC(q[1]), sb);
      pq(3);
      sa = MS16(kf[0][2], BC(q[2]), sa); sb = MS16(kf[1][2], BC(q[2]), sb);
      sa = MS16(kf[0][3], BC(q[3]), sa); sb = MS16(kf[1][3], BC(q[3]), sb);
    }
    CB(10);
    {
      bf16x8 vt[8], wt[16]; f32x4 ot[8], y[4]; v4u of[4];
      auto rT = [&](int n) { vt[n] = trd(a9[n >> 1][0] + 8 * (n & 1), a9[n >> 1][1] + 8 * (n & 1)); };
      auto rN = [&](int n) { const int ks = n >> 2, c = 4 * (ks >> 1) + (n & 3); wt[n] = trd(aq[c][0] + (2 + (ks & 1)) * PG, aq[c][1] + (2 + (ks & 1)) * PG); };
      rT(0); rT(1); rT(2); rT(3); SB();
      float mx, m2;
      asm volatile("s_nop 1\n\tv_max3_f32 %0, %2, %3, %4\n\tv_max3_f32 %1, %5, %6, %7\n\tv_max3_f32 %0, %0, %8, %9\n\tv_max_f32 %0, %0, %1\n\t"
                   "v_mov_b32 %1, %0\n\ts_nop 1\n\tv_permlane16_swap_b32 %0, %1\n\tv_max_f32 %0, %0, %1\n\t"
                   "v_mov_b32 %1, %0\n\ts_nop 1\n\tv_permlane32_swap_b32 %0, %1\n\tv_max_f32 %0, %0, %1"
                   : "=&v"(mx), "=&v"(m2) : "v"(sa[0]), "v"(sa[1]), "v"(sa[2]), "v"(sa[3]), "v"(sb[0]), "v"(sb[1]), "v"(sb[2]), "v"(sb[3]));
      const float c = 0.08838834764831845f * 1.4426950408889634f, mc = -mx * c;
      float e[8], z2;
#pragma unroll
      for (int n = 0; n < 4; ++n) { e[n] = __builtin_amdgcn_exp2f(__builtin_fmaf(sa[n], c, mc)); e[4 + n] = __builtin_amdgcn_exp2f(__builtin_fmaf(sb[n], c, mc)); }
      float z = ((e[0] + e[2]) + (e[4] + e[6])) + ((e[1] + e[3]) + (e[5] + e[7]));
      asm volatile("v_mov_b32 %1, %0\n\ts_nop 1\n\tv_permlane16_swap_b32 %0, %1\n\tv_add_f32 %0, %0, %1\n\t"
                   "v_mov_b32 %1, %0\n\ts_nop 1\n\tv_permlane32_swap_b32 %0, %1\n\tv_add_f32 %0, %0, %1" : "+v"(z), "=&v"(z2));
      const float r = __builtin_amdgcn_rcpf(z);
      const v4u pv = {pk(e[0] * r, e[1] * r), pk(e[2] * r, e[3] * r), pk(e[4] * r, e[5] * r), pk(e[6] * r, e[7] * r)};
      st16(pv, v16, rw, sp0);
      rT(4); rT(5); rT(6); rT(7);
      v2u ck;
#pragma unroll
      for (int n = 0; n < 8; ++n) {
        ot[n] = MS16(vt[n], BC(pv), z4);
        if (n == 0) { ck = row8(fold(pv)); PIN(ck); } else if (n == 1) { ck = half8(ck); ck.x += tag; st8(ck, vcs, rw, sp1); }
        else if (n >= 4) { rV(0, n - 4); rW(n - 4, 14 + ((n - 4) >> 1)); }
      }
      STAMP(61);
      CB(11);
#pragma unroll
      for (int s = 0; s < 4; ++s) {
        rN(2 * s); rN(2 * s + 1);
        of[s] = v4u{pk(ot[2 * s][0], ot[2 * s][1]), pk(ot[2 * s][2], ot[2 * s][3]), pk(ot[2 * s + 1][0], ot[2 * s + 1][1]), pk(ot[2 * s + 1][2], ot[2 * s + 1][3])};
        PIN(of[s]);
        u = MF32(vf[s], bw[s], s ? u : zero);
      }
#pragma unroll
      for (int n = 0; n < 16; ++n) {
        y[n & 3] = MS16(wt[n], BC(of[n >> 2]), n < 4 ? z4 : y[n & 3]);
        if (n < 8) rN(n + 8); else if (n & 1) rW(4 + ((n - 8) >> 1), 9 + ((n - 8) >> 2)); else rV(0, 4 + ((n - 8) >> 1));
      }
      v4u e0, e1; v2u g0, g1;
      CB(12);
      lagW(1, 4, 5, 6, 7, [&](int s) {
        if (s == 0) { e0 = v4u{pk(y[0][0], y[0][1]), pk(y[0][2], y[0][3]), pk(y[1][0], y[1][1]), pk(y[1][2], y[1][3])}; st16(e0, v9, rw, s9a); }
        else if (s == 1) { e1 = v4u{pk(y[2][0], y[2][1]), pk(y[2][2], y[2][3]), pk(y[3][0], y[3][1]), pk(y[3][2], y[3][3])}; st16(e1, v9, rw, s9b); }
        else if (s == 2) { g0 = fold(e0); PIN(g0); } else if (s == 3) { g1 = fold(e1); PIN(g1); }
        else if (s == 4) { g0 = row8(g0); g1 = row8(g1); g0 = col < 8 ? g0 : g1; PIN(g0); }
        else if (s == 5) { g0 = half8(g0); g0.x += tag; st8(g0, vc9, rw, s9c); } });
    }
    CB(13); lagW(2, 11, 0, 1, 8, nop);
    CB(14); lagW(3, 12, 13, 14, 15, nop);
#pragma unroll
    for (int s = 4; s < 8; ++s) u = MF32(vf[s], bw[s], u);
    CB(15);
  }
  {
    const u32 so = ((bp << 5) | (i << 3) | (bb * 4 + (ct >> 1))) * SLAB;
    const u32 sd0 = __builtin_amdgcn_readfirstlane(so + hq * 2048 + (ct & 1) * 1024), sd1 = sd0 + 2 * SLAB;
    const u32 sc0 = __builtin_amdgcn_readfirstlane(so + CMAIN + hq * 128 + (ct & 1) * 32);
    bf16x8 pf[8];
#pragma unroll
    for (int n = 0; n < 8; ++n) { pf[n] = rd16(ap + (n >> 1) * 4096 + (n & 1) * 1024); SB(); }
    uu[3][0] = pack8<0>(u); uu[3][1] = pack8<1>(u);
    f32x16 o = zero;
#pragma unroll
    for (int n = 0; n < 8; ++n) o = MF32(pf[n], BC(uu[n >> 1][n & 1]), o);
    const v4u d0 = pack8<0>(o); st16(d0, v16, rw, sd0);
    const v4u d1 = pack8<1>(o); st16(d1, v16, rw, sd1);
    const v2u f0 = row8(fold(d0)), f1 = row8(fold(d1));
    v2u f = half8(col < 8 ? f0 : f1); f.x += tag; st8(f, vcm, rw, sc0);
  }
  STAMP(58);
  if (w == 7) {
    const u32 vd = v16 + b * SLAB + IN9, vc = row * 8 + b * SLAB + C9;
    v4u d[8]; v2u c[8];
    _Pragma("nounroll") for (int sp = 0; sp < SPIN_CAP; ++sp) {
      asm volatile("buffer_load_dwordx4 %0, %16, %18, 0 offen sc1\n\tbuffer_load_dwordx4 %1, %16, %18, 0 offen offset:1024 sc1\n\t"
                   "buffer_load_dwordx4 %2, %16, %18, 0 offen offset:2048 sc1\n\tbuffer_load_dwordx4 %3, %16, %18, 0 offen offset:3072 sc1\n\t"
                   "buffer_load_dwordx4 %4, %16, %18, %19 offen sc1\n\tbuffer_load_dwordx4 %5, %16, %18, %19 offen offset:1024 sc1\n\t"
                   "buffer_load_dwordx4 %6, %16, %18, %19 offen offset:2048 sc1\n\tbuffer_load_dwordx4 %7, %16, %18, %19 offen offset:3072 sc1\n\t"
                   "buffer_load_dwordx2 %8, %17, %18, 0 offen sc1\n\tbuffer_load_dwordx2 %9, %17, %18, 0 offen offset:32 sc1\n\t"
                   "buffer_load_dwordx2 %10, %17, %18, 0 offen offset:64 sc1\n\tbuffer_load_dwordx2 %11, %17, %18, 0 offen offset:96 sc1\n\t"
                   "buffer_load_dwordx2 %12, %17, %18, 0 offen offset:128 sc1\n\tbuffer_load_dwordx2 %13, %17, %18, 0 offen offset:160 sc1\n\t"
                   "buffer_load_dwordx2 %14, %17, %18, 0 offen offset:192 sc1\n\tbuffer_load_dwordx2 %15, %17, %18, 0 offen offset:224 sc1\n\t"
                   "s_waitcnt vmcnt(0)"
                   : "=&v"(d[0]), "=&v"(d[1]), "=&v"(d[2]), "=&v"(d[3]), "=&v"(d[4]), "=&v"(d[5]), "=&v"(d[6]), "=&v"(d[7]),
                     "=&v"(c[0]), "=&v"(c[1]), "=&v"(c[2]), "=&v"(c[3]), "=&v"(c[4]), "=&v"(c[5]), "=&v"(c[6]), "=&v"(c[7])
                   : "v"(vd), "v"(vc), "s"(rw), "s"(4096) : "memory");
      const v2u f0 = fold(d[3], fold(d[2], fold(d[1], fold(d[0])))), f1 = fold(d[7], fold(d[6], fold(d[5], fold(d[4]))));
      const v2u f = half8(row8(v2u{f0.x + f1.x, f0.y ^ f1.y}));
      u32 cs = 0, cx = 0;
#pragma unroll
      for (int n = 0; n < 8; ++n) { cs += c[n].x; cx ^= c[n].y; }
      if (__all(f.x + 8 * tag == cs && f.y == cx)) break;
      __builtin_amdgcn_s_sleep(2);
    }
    f32x4 y = {0.f, 0.f, 0.f, 0.f};
#pragma unroll
    for (int n = 0; n < 8; ++n) y = MF16(BC(sel), BC(d[n]), y);
    __builtin_amdgcn_raw_buffer_store_b64(v2u{pk(y[0], y[1]), pk(y[2], y[3])}, ry,
                                          row < 2 ? (64 * bp + 16 * (hq >> 1) + col) * 1152 + row * 32 + (512 + 32 * (hq & 1) + 4 * i) * 2 : OOB, 0, 0);
    if (me == 0) __builtin_amdgcn_raw_buffer_store_b32(tag, rw, lane == 0 ? bp * (32 * SLAB) + EPOCH : OOB, 0, 16);
  } else {
    asm volatile("s_waitcnt vmcnt(0)" ::: "memory");
  }
  STAMP(59);
  {
    v4u ga, gb; v2u ca, cb;
    _Pragma("nounroll") for (int sp = 0; sp < SPIN_CAP; ++sp) {
      asm volatile("buffer_load_dwordx4 %0, %4, %7, 0 offen sc1\n\tbuffer_load_dwordx4 %1, %4, %7, %5 offen sc1\n\t"
                   "buffer_load_dwordx2 %2, %6, %7, 0 offen sc1\n\tbuffer_load_dwordx2 %3, %6, %7, 0 offen offset:512 sc1\n\ts_waitcnt vmcnt(0)"
                   : "=&v"(ga), "=&v"(gb), "=&v"(ca), "=&v"(cb) : "v"(vg), "s"(8192), "v"(vgc), "s"(rw) : "memory");
      const v2u f = half8(row8(fold(gb, fold(ga))));
      if (__all(f.x + 2 * tag == ca.x + cb.x && f.y == (ca.y ^ cb.y))) break;
      __builtin_amdgcn_s_sleep(1);
    }
    f32x4 y = {0.f, 0.f, 0.f, 0.f};
    y = MF16(BC(ga), BC(sel), y); y = MF16(BC(gb), BC(sel), y);
    __builtin_amdgcn_raw_buffer_store_b64(v2u{pk(y[0], y[1]), pk(y[2], y[3])}, ry, vy,
                                          4 * ((w >> 1) & 1) * 1152 + (128 * i + 64 * (hq & 1) + 32 * (w >> 2) + 16 * (w & 1)) * 2, 0);
  }
  STAMP(60);
  { const u64 until = dg_t0 + 4000 + (w == 0 && blockIdx.x == 77 ? 4 * (dg_tb - dg_ta) : 0); while (__builtin_amdgcn_s_memrealtime() < until && __builtin_amdgcn_s_memrealtime() < dg_t0 + 20000) __builtin_amdgcn_s_sleep(1); }
}

extern "C" void kernel_launch(void* const* d_in, const int* in_sizes, int n_in, void* d_out, int out_size, void* d_ws, size_t ws_size,
                              hipStream_t stream) {
  gqa_fused<<<256, 512, 0, stream>>>(d_in[0], d_in[1], d_in[2], d_in[3], d_in[4], d_out, (char*)d_ws
  );
}
```

```cpp
#include <hip/hip_runtime.h>
#include <hip/hip_bf16.h>

typedef unsigned long long u64;
typedef unsigned u32;
typedef u32 v4u __attribute__((ext_vector_type(4)));
typedef u32 v2u __attribute__((ext_vector_type(2)));
typedef short s16x4 __attribute__((ext_vector_type(4)));
typedef short bf16x8 __attribute__((ext_vector_type(8)));
typedef float f32x16 __attribute__((ext_vector_type(16)));
typedef float f32x4 __attribute__((ext_vector_type(4)));
typedef float f32x2 __attribute__((ext_vector_type(2)));
typedef __bf16 b16x2 __attribute__((ext_vector_type(2)));
#define LAS __attribute__((address_space(3)))
#define GAS __attribute__((address_space(1)))
#define DI __device__ __forceinline__
typedef __amdgpu_buffer_rsrc_t rsrc_t;

constexpr u32 PG = 8192;
constexpr u32 SLAB = 32768;
constexpr u32 IN9 = 16384, POUT = 24576, CMAIN = 28672, C9 = 29696, CP = 30208, EPOCH = 31744;
constexpr u32 WS_BYTES = 256 * SLAB;
constexpr u32 OOB = 0x80000000u;
constexpr int SPIN_CAP = 1 << 16;
#define CODE_BYTES 17920
#define PACE 6
#define PSLEEP 1
#define KPOS 0
#define SH0 1
#define SH1 2
#define SH2 3
#define SH3 4
#define HPRIO 2
#define PPOS 3
#define GSLEEP 20

#define STAMP(i)
#define SB() __builtin_amdgcn_sched_barrier(0)
#define HT(k, N) asm volatile("s_waitcnt vmcnt(%0) lgkmcnt(0)\n\ts_barrier" :: "i"(N) : "memory")
#define CT(k) do { SB(); asm volatile("s_waitcnt lgkmcnt(0)\n\ts_barrier" ::: "memory"); SB(); } while (0)
#define CB(k) do { SB(); asm volatile("s_barrier" ::: "memory"); SB(); } while (0)
#define HV(k, N) asm volatile("s_waitcnt vmcnt(%0)\n\ts_barrier" :: "i"(N) : "memory")
#define WV(N) asm volatile("s_waitcnt vmcnt(%0)" :: "i"(N) : "memory")
#define PIN(v) asm volatile("" : "+v"(v))

template <bool NT> DI void dma(u32 lds, u32 voff, rsrc_t rs, u32 soff) {
  if constexpr (NT) asm volatile("s_mov_b32 m0, %0\n\ts_nop 0\n\tbuffer_load_dwordx4 %1, %2, %3 offen nt lds" :: "s"(lds), "v"(voff), "s"(rs), "s"(soff) : "memory");
  else              asm volatile("s_mov_b32 m0, %0\n\ts_nop 0\n\tbuffer_load_dwordx4 %1, %2, %3 offen lds" :: "s"(lds), "v"(voff), "s"(rs), "s"(soff) : "memory");
}
DI void st16(v4u v, u32 voff, rsrc_t rs, u32 soff) {
  asm volatile("buffer_store_dwordx4 %0, %1, %2, %3 offen sc1\n\ts_nop 1" :: "v"(v), "v"(voff), "s"(rs), "s"(soff) : "memory");
}
DI void st8(v2u v, u32 voff, rsrc_t rs, u32 soff) {
  asm volatile("buffer_store_dwordx2 %0, %1, %2, %3 offen sc1" :: "v"(v), "v"(voff), "s"(rs), "s"(soff) : "memory");
}
DI s16x4 tr4(u32 a) { return __builtin_amdgcn_ds_read_tr16_b64_v4i16((LAS s16x4*)(size_t)a); }
DI bf16x8 trd(u32 a0, u32 a1) { return __builtin_shufflevector(tr4(a0), tr4(a1), 0, 1, 2, 3, 4, 5, 6, 7); }
DI bf16x8 rd16(u32 a) { return *(LAS bf16x8*)(size_t)a; }
DI u32 pk(float a, float b) { f32x2 v = {a, b}; return __builtin_bit_cast(u32, __builtin_convertvector(v, b16x2)); }
template <int S> DI v4u pack8(const f32x16& c) {
  v4u r = {pk(c[8 * S], c[8 * S + 1]), pk(c[8 * S + 2], c[8 * S + 3]), pk(c[8 * S + 4], c[8 * S + 5]), pk(c[8 * S + 6], c[8 * S + 7])};
  PIN(r);
  return r;
}
DI f32x16 MF32(bf16x8 a, bf16x8 b, f32x16 c) { SB(); f32x16 d = __builtin_amdgcn_mfma_f32_32x32x16_bf16(a, b, c, 0, 0, 0); PIN(d); SB(); return d; }
DI f32x4 MS16(bf16x8 a, bf16x8 b, f32x4 c) { SB(); f32x4 d = __builtin_amdgcn_mfma_f32_16x16x32_bf16(a, b, c, 0, 0, 0); PIN(d); SB(); return d; }
#define MF16(a, b, c) __builtin_amdgcn_mfma_f32_16x16x32_bf16((a), (b), (c), 0, 0, 0)
#define BC(v) __builtin_bit_cast(bf16x8, (v))
template <int C> DI float dppf(float v) { return __builtin_bit_cast(float, __builtin_amdgcn_mov_dpp(__builtin_bit_cast(int, v), C, 0xF, 0xF, true)); }
template <int C> DI u32 dppu(u32 v) { return (u32)__builtin_amdgcn_mov_dpp((int)v, C, 0xF, 0xF, true); }
DI u32 xor3(u32 a, u32 b, u32 c) { return __builtin_amdgcn_bitop3_b32(a, b, c, 0x96); }
DI v2u fold(v4u d) { return v2u{(d.x + d.y + d.z) + d.w, xor3(d.x, d.y, d.z) ^ d.w}; }
DI v2u fold(v4u d, v2u a) { return v2u{(d.x + d.y + a.x) + d.z + d.w, xor3(xor3(d.x, d.y, a.y), d.z, d.w)}; }
DI v2u row8(v2u v) { return v2u{v.x + dppu<0x128>(v.x), v.y ^ dppu<0x128>(v.y)}; }
DI v2u half8(v2u v) {
  v.x += dppu<0x141>(v.x); v.y ^= dppu<0x141>(v.y); v.x += dppu<0x4E>(v.x); v.y ^= dppu<0x4E>(v.y);
  v.x += dppu<0xB1>(v.x); v.y ^= dppu<0xB1>(v.y);
  return v;
}

__global__ void __launch_bounds__(512, 2)
gqa_fused(const void* __restrict__ x, const void* __restrict__ kc, const void* __restrict__ vc, const void* __restrict__ wq,
          const void* __restrict__ wo, void* __restrict__ out, char* __restrict__ ws
) {
  __shared__ __attribute__((aligned(1024))) char smem[20 * PG];
  u64 pc0;
  asm volatile("s_getpc_b64 %0" : "=s"(pc0));
  const u32 L = (u32)(size_t)(LAS char*)smem;
  const u32 tid = threadIdx.x, lane = tid & 63;
  const u32 w = __builtin_amdgcn_readfirstlane(tid >> 6);
  const u32 b = blockIdx.x, hq = b & 7, bp = b >> 5, i = (b >> 3) & 3, h = 4 * hq + i;
  u64 dg_t0; asm volatile("s_memrealtime %0\n\ts_waitcnt lgkmcnt(0)" : "=s"(dg_t0) :: "memory");
  const rsrc_t rw = __builtin_amdgcn_make_buffer_rsrc((void*)ws, 0, WS_BYTES, 0x00020000);
  const rsrc_t ry = __builtin_amdgcn_make_buffer_rsrc(out, 0, 512 * 1152, 0x00020000);
  const u32 me = hq * 4 + i, row = lane >> 4, col = lane & 15;
  const u32 hh = lane >> 5, l31 = lane & 31;
  const u32 ct = w & 3, bb = w >> 2, q4 = col >> 2, p4 = lane & 3;
  f32x16 zero;
#pragma unroll
  for (int n = 0; n < 16; ++n) zero[n] = 0.f;
  u32 tag, vy, vg, vgc, v16, vcm, ap, aw[2][2], av[8]; v4u sel;
  bf16x8 vf[8], bw[8]; v4u uu[4][2]; f32x16 u;
  auto setup2 = [&]() {
    const u32 one = (col & 1) ? 0x3F800000u : 0x00003F80u;
    sel = v4u{(col >> 1) == 0 ? one : 0u, (col >> 1) == 1 ? one : 0u, (col >> 1) == 2 ? one : 0u, (col >> 1) == 3 ? one : 0u};
    const u32 yrow = 64 * bp + 32 * (hq >> 2) + 16 * ((hq >> 1) & 1) + (col & 3) + 8 * ((col >> 2) & 1);
    vy = col < 8 ? yrow * 1152 + 8 * row : OOB;
    vg = row * 2048 + w * 256 + col * 16 + b * SLAB; vgc = row * 128 + w * 8 + b * SLAB + CMAIN;
    v16 = lane * 16; vcm = (col & 7) == 0 ? (col >> 3) * (2 * SLAB) + row * 8 : OOB;
    ap = L + 2 * PG + bb * 2048 + lane * 16;
    PIN(sel); PIN(vy); PIN(vg); PIN(vgc); PIN(v16); PIN(vcm); PIN(ap);
#pragma unroll
    for (int t = 0; t < 2; ++t) {
      aw[0][t] = L + 256 * (8 * hh + 4 * t + q4) + 16 * ((4 * ct + 2 * (row & 1) + (p4 >> 1)) ^ ((q4 << 2) | (2 * hh + t))) + 8 * (p4 & 1);
      aw[1][t] = aw[0][t] + 8 * PG;
      PIN(aw[0][t]); PIN(aw[1][t]);
    }
    const u32 arow = 256 * l31 + 16 * (hh ^ (((l31 & 3) << 2) | ((l31 >> 2) & 3)));
#pragma unroll
    for (int n = 0; n < 8; ++n) { av[n] = L + (16 + bb) * PG + (arow ^ (128 * (bp & 1)) ^ (32 * n)); PIN(av[n]); }
  };
  auto rV = [&](int slot, int s) { vf[s] = rd16(av[s] + 2 * slot * PG); };
  auto rW = [&](int s, int pg) { const u32 o = (pg & 7) * PG + (s & 1) * 4096; bw[s] = trd(aw[pg >> 3][0] + o, aw[pg >> 3][1] + o); };
  auto headW = [&](int hs, int p0, int p1, int p2, int p3, auto f) {
    const int pg[4] = {p0, p1, p2, p3};
    const f32x16 o = u;
    rW(0, p0); SB(); rW(1, p0); SB(); rW(2, p1); SB(); rW(3, p1); SB();
    uu[hs - 1][0] = pack8<0>(o); uu[hs - 1][1] = pack8<1>(o);
#pragma unroll
    for (int s = 0; s < 8; ++s) {
      u = MF32(vf[s], bw[s], s ? u : zero);
      if (s < 4) rW(s + 4, pg[2 + (s >> 1)]);
      if (hs == 1) rV(1, s);
      f(s);
    }
  };
  auto lagW = [&](int hs, int p0, int p1, int p2, int p3, auto f) {
    const int pg[4] = {p0, p1, p2, p3};
#pragma unroll
    for (int s = 4; s < 8; ++s) { u = MF32(vf[s], bw[s], u); rW(s - 4, pg[(s - 4) >> 1]); if (hs == 2) rV(1, s); f(s - 4); }
    const f32x16 o = u;
#pragma unroll
    for (int s = 0; s < 4; ++s) {
      u = MF32(vf[s], bw[s], s ? u : zero);
      rW(s + 4, pg[2 + (s >> 1)]);
      if (hs == 1) rV(1, s);
      if (s == 2) uu[hs - 1][0] = pack8<0>(o);
      if (s == 3) uu[hs - 1][1] = pack8<1>(o);
      f(s + 4);
    }
  };
  auto nop = [](int) {};
  asm volatile("" :: "s"(x), "s"(kc), "s"(vc), "s"(wq), "s"(wo), "s"(out), "s"(ws));

  if (__builtin_expect(w >= 4, 1)) {
    const u32 g = w - 4;
    const rsrc_t rx = __builtin_amdgcn_make_buffer_rsrc((void*)x, 0, 512 * 1152, 0x00020000);
    const rsrc_t rk = __builtin_amdgcn_make_buffer_rsrc((void*)kc, 0, 256 * 8192, 0x00020000);
    const rsrc_t rv = __builtin_amdgcn_make_buffer_rsrc((void*)vc, 0, 256 * 8192, 0x00020000);
    const rsrc_t rq = __builtin_amdgcn_make_buffer_rsrc((void*)wq, 0, 576 * 8192, 0x00020000);
    const rsrc_t ro = __builtin_amdgcn_make_buffer_rsrc((void*)wo, 0, 4096 * 1152, 0x00020000);
    const u32 xr = 8 * g + (lane >> 3);
    const u32 vx = xr * 1152 + 16 * ((lane & 7) ^ ((xr >> 1) & 7));
    const u32 pr = 4 * g + row;
    const u32 pc = col ^ ((row << 2) | g);
    const u32 vq = pr * 8192 + 16 * pc;
    const u32 Lg = L + 1024 * g;
    const u32 r0 = 3 * ((bp + i) % 3);
    const u32 sx = bp * (64 * 1152), sq = 256 * h;
    auto step = [&](int s) {
      u32 ks = r0 + s; ks = ks >= 9 ? ks - 9 : ks;
      const u32 o = sx + ks * 128, o2 = sq + ks * (64 * 8192);
      dma<false>(Lg + (8 + (s & 3)) * PG, vx, rx, o); dma<false>(Lg + (8 + (s & 3)) * PG + 4096, vx, rx, o + 32 * 1152);
#pragma unroll
      for (int n = 0; n < 4; ++n) dma<true>(Lg + 2 * (s & 3) * PG + n * 4096, vq, rq, o2 + n * (16 * 8192));
    };
    step(0); step(1);
    {
      const rsrc_t rc = __builtin_amdgcn_make_buffer_rsrc((void*)(pc0 & ~255ull), 0, CODE_BYTES, 0x00020000);
      asm volatile("s_buffer_load_dword s100, %0, %1\n\ts_buffer_load_dword s101, %2, %3\n\ts_buffer_load_dword s101, %2, %3 offset:64\n\t"
                   "s_buffer_load_dword s101, %2, %3 offset:128\n\ts_buffer_load_dword s101, %4, %5"
                   :: "s"(rw), "s"(bp * (32 * SLAB) + EPOCH), "s"(rc), "s"(((b >> 3) * 4 + g) * 192), "s"(ry), "s"(bp * (64 * 1152)) : "s100", "s101", "memory");
    }
    u32 vkv = pr * 256 + 16 * pc, vo = pr * 1152 + 16 * pc, vo9 = (pr + 64 * (pc >> 3)) * 1152 + 16 * (pc & 7);
    u32 pl = L + 2 * PG + g * 4096 + row * 512 + col * 16;
    u32 vcl = row * 8;
    PIN(vkv); PIN(vo); PIN(vo9); PIN(pl); PIN(vcl);
    u32 bs = b;
    asm volatile("" : "+s"(bs));
    setup2();
    auto ldk = [&](u32 n) {
      const u32 o = ((2 * bp + n) * 16 + (h >> 1)) * 8192;
      dma<true>(Lg + (12 + n) * PG, vkv, rk, o); dma<true>(Lg + (12 + n) * PG + 4096, vkv, rk, o + 4096);
    };
    auto ldv = [&](u32 n) {
      const u32 o = ((2 * bp + (n & 1)) * 16 + 2 * hq + ((n >> 1) ^ (bp >> 2))) * 8192;
      dma<true>(Lg + (16 + n) * PG, vkv, rv, o); dma<true>(Lg + (16 + n) * PG + 4096, vkv, rv, o + 4096);
    };
    auto ldw1 = [&](int n, int pg, int k) {
      dma<true>(Lg + pg * PG + k * 4096, vo, ro, ((bs & 7) * 512 + 32 * (n ^ ((bs >> 5) << 1)) + 16 * k) * 1152 + 256 * ((bs >> 3) & 3));
    };
    auto ldw = [&](int n, int pg) { ldw1(n, pg, 0); ldw1(n, pg, 1); };
    auto ldn = [&](int n, int pg) {
      const u32 o = (h * 128 + 32 * n) * 1152 + 1024;
      dma<true>(Lg + pg * PG, vo9, ro, o); dma<true>(Lg + pg * PG + 4096, vo9, ro, o + 16 * 1152);
    };
    auto extra = [&](int t) {
      if (t == KPOS) { ldk(i & 1); ldk((i & 1) ^ 1); }
      const u32 grp = bp & 3;
      if ((t == SH0 && grp == 0) || (t == SH1 && grp == 1) || (t == SH2 && grp == 2) || (t == SH3 && grp == 3)) {
        ldv(i); ldw(0, 14); ldw(1, 15);
        dma<true>(Lg + (16 + (i ^ 1)) * PG, vo9, ro, (h * 128 + 16 * (bp & 3)) * 1152 + 1024);
      }
    };
    step(2);
    HV(0, 12); step(3); extra(0);
    HV(1, 12); step(4); extra(1);
    HV(2, 12); step(5); extra(2);
    HV(3, 12); step(6); extra(3);
    HV(4, 12); step(7); extra(4);
    HV(5, 12); step(8);
    HV(6, 12); ldv(i ^ 1); ldv(i ^ 2);
    HV(7, 10); ldv(i ^ 3);
    asm volatile("s_waitcnt lgkmcnt(0)\n\ts_mov_b32 %0, s100" : "=s"(tag) :: "memory");
    tag += 1;
    HV(8, 8);  ldw(2, 9);
    HV(9, 8);  WV(PACE); ldw(3, 10); WV(PACE); ldn(0, 2); WV(PACE); ldn(1, 3);
    HV(10, 8);
    rV(0, 0); rW(0, 14); SB(); rV(0, 1); rW(1, 14); SB(); rV(0, 2); rW(2, 15); SB(); rV(0, 3); rW(3, 15);
#pragma unroll
    for (int s = 0; s < 4; ++s) { u = MF32(vf[s], bw[s], s ? u : zero); rV(0, s + 4); }
    WV(PACE); ldw(4, 4); WV(PACE); ldw(5, 5); WV(PACE); ldw(6, 6); WV(PACE); ldw(7, 7);
    HV(11, 8);
    rW(4, 9); SB(); rW(5, 9); SB(); rW(6, 10); SB(); rW(7, 10); SB();
    ldw(8, 11);
#pragma unroll
    for (int s = 4; s < 8; ++s) u = MF32(vf[s], bw[s], u);
    WV(PACE); ldw(9, 0); WV(PACE); ldw(10, 1); WV(PACE); ldw(11, 8);
    __builtin_amdgcn_s_setprio(HPRIO);
    HV(12, 8);
    headW(1, 4, 5, 6, 7, [&](int s) { ldw1(12 + (s >> 1), 12 + (s >> 1), s & 1); });
    HV(13, 8);
    v4u p0, p1, p2, p3; v2u c0, c1, c2, c3;
    const u32 spl = ((bp << 5) | ((g ^ (bp >> 1)) << 3) | hq) * SLAB;
#define LOAD_P "buffer_load_dwordx4 %0, %8, %10, %11 offen sc1\n\tbuffer_load_dwordx4 %1, %8, %10, %11 offen offset:1024 sc1\n\t" \
               "buffer_load_dwordx4 %2, %8, %10, %11 offen offset:2048 sc1\n\tbuffer_load_dwordx4 %3, %8, %10, %11 offen offset:3072 sc1\n\t" \
               "buffer_load_dwordx2 %4, %9, %10, %12 offen sc1\n\tbuffer_load_dwordx2 %5, %9, %10, %12 offen offset:32 sc1\n\t" \
               "buffer_load_dwordx2 %6, %9, %10, %12 offen offset:64 sc1\n\tbuffer_load_dwordx2 %7, %9, %10, %12 offen offset:96 sc1"
#define P_ARGS "=&v"(p0), "=&v"(p1), "=&v"(p2), "=&v"(p3), "=&v"(c0), "=&v"(c1), "=&v"(c2), "=&v"(c3) : "v"(v16), "v"(vcl), "s"(rw), "s"(spl + POUT), "s"(spl + CP) : "memory"
    headW(2, 11, 0, 1, 8, [&](int s) { if (s == PPOS) asm volatile(LOAD_P : P_ARGS); });
    HV(14, 8);
    headW(3, 12, 13, 14, 15, nop);
    asm volatile("s_waitcnt vmcnt(0)" : "+v"(p0), "+v"(p1), "+v"(p2), "+v"(p3), "+v"(c0), "+v"(c1), "+v"(c2), "+v"(c3) :: "memory");
    STAMP(62);
    _Pragma("nounroll") for (int sp = 0; sp < SPIN_CAP; ++sp) {
      *(LAS v4u*)(size_t)pl = p0; *(LAS v4u*)(size_t)(pl + 256) = p1; *(LAS v4u*)(size_t)(pl + 2048) = p2; *(LAS v4u*)(size_t)(pl + 2304) = p3;
      const v2u f0 = fold(p1, fold(p0)), f1 = fold(p3, fold(p2));
      const v2u f = half8(row8(v2u{f0.x + f1.x, f0.y ^ f1.y}));
      if (__all(f.x + 4 * tag == (c0.x + c1.x) + (c2.x + c3.x) && f.y == (xor3(c0.y, c1.y, c2.y) ^ c3.y))) break;
      asm volatile("s_waitcnt lgkmcnt(0)\n\t" LOAD_P "\n\ts_waitcnt vmcnt(0)" : P_ARGS);
    }
    HT(15, 0);
  } else {
    setup2();
    const u32 mt = w >> 1;
    u32 aq[8][2], ax[2], ak[8], a9[4][2];
    const u32 xrow = 16 * w + col, key = 16 * (col >> 3) + (col & 7);
    ax[0] = L + 8 * PG + 128 * xrow + 16 * (row ^ ((xrow >> 1) & 7)); ax[1] = ax[0] ^ 64;
    PIN(ax[0]); PIN(ax[1]);
#pragma unroll
    for (int n = 0; n < 8; ++n) {
#pragma unroll
      for (int t = 0; t < 2; ++t) {
        aq[n][t] = L + 256 * (8 * row + 4 * t + q4) + 16 * ((2 * n + (p4 >> 1)) ^ ((q4 << 2) | ((2 * row + t) & 3))) + 8 * (p4 & 1);
        PIN(aq[n][t]);
      }
      ak[n] = L + (12 + mt) * PG + 256 * key + 16 * ((2 * n + (row >> 1)) ^ (((key & 3) << 2) | ((key >> 2) & 3))) + 8 * (row & 1);
      a9[n >> 1][n & 1] = L + (16 + mt + 2 * ((i >> 1) ^ (bp >> 2))) * PG + 256 * (16 * (row >> 1) + 4 * (row & 1) + 8 * (n & 1) + q4) +
                          16 * ((4 * (n >> 1) + p4) ^ ((q4 << 2) | ((row & 1) + 2 * (n & 1))));
      PIN(ak[n]); PIN(a9[n >> 1][n & 1]);
    }
    u32 v9 = (row << 3) * SLAB + col * 16, vc9 = (col & 7) == 0 ? (row << 3) * SLAB + (col >> 3) * SLAB : OOB, vcs = col == 0 ? row * 8 : OOB;
    PIN(v9); PIN(vc9); PIN(vcs);
    const u32 sp0 = b * SLAB + POUT + w * 1024, sp1 = b * SLAB + CP + w * 32;
    const u32 s9 = ((bp << 5) | (2 * w)) * SLAB;
    const u32 s9a = s9 + IN9 + me * 256, s9b = s9a + SLAB, s9c = s9 + C9 + me * 8;
    asm volatile("" :: "s"(sp0), "s"(sp1), "s"(s9a), "s"(s9b), "s"(s9c));

    const f32x4 z4 = {0.f, 0.f, 0.f, 0.f};
    f32x4 qa[8];
#pragma unroll
    for (int n = 0; n < 8; ++n) qa[n] = z4;
    bf16x8 A[16], B[2], kf[2][4];
    auto rA = [&](int sl, int n) { const u32 o = (2 * sl + (n >> 3)) * PG; A[n] = trd(aq[n & 7][0] + o, aq[n & 7][1] + o); };
    auto rB = [&](int sl, int k) { B[k] = rd16(ax[k] + sl * PG); };
    auto rK = [&](int T, int ks) {
      const s16x4 lo = *(LAS s16x4*)(size_t)(ak[2 * ks + T] + T * 2048), hi = *(LAS s16x4*)(size_t)(ak[2 * ks + 1 - T] + T * 2048);
      kf[T][ks] = __builtin_shufflevector(lo, hi, 0, 1, 2, 3, 4, 5, 6, 7);
    };
    bf16x8 Bc;
    auto head = [&](int sl, bool carry) {
      rB(sl, 0);
#pragma unroll
      for (int n = 0; n < 6; ++n) { rA(sl, n); if (carry) qa[n + 2] = MS16(A[n + 10], Bc, qa[n + 2]); else SB(); }
      rB(sl, 1);
    };
    auto p1 = [&](int t) {
      head(t & 3, t > 0);
#pragma unroll
      for (int n = 0; n < 10; ++n) { qa[n & 7] = MS16(A[n], B[n >> 3], qa[n & 7]); rA(t & 3, n + 6); }
      Bc = B[1];
      if (t == KPOS + 4) { rK(0, 0); rK(0, 1); rK(0, 2); rK(0, 3); }
      if (t == KPOS + 5) { rK(1, 0); rK(1, 1); rK(1, 2); rK(1, 3); }
    };
    CT(0); p1(0); CT(1); p1(1); CT(2); p1(2); CT(3); p1(3);
    asm volatile("s_buffer_load_dword %0, %1, %2\n\ts_waitcnt lgkmcnt(0)" : "=s"(tag) : "s"(rw), "s"(bp * (32 * SLAB) + EPOCH) : "memory");
    tag += 1;
    CT(4); p1(4); CT(5); p1(5); CB(6); p1(6); CB(7); p1(7);
    CB(8);
    head(0, true);
#pragma unroll
    for (int n = 0; n < 8; ++n) { qa[n] = MS16(A[n], B[0], qa[n]); if (n < 2) rA(0, n + 6); }
    CB(9);
    f32x4 sa = z4, sb = z4;
    {
      v4u q[4];
      auto pq = [&](int ks) { q[ks] = v4u{pk(qa[2 * ks][0], qa[2 * ks][1]), pk(qa[2 * ks][2], qa[2 * ks][3]), pk(qa[2 * ks + 1][0], qa[2 * ks + 1][1]), pk(qa[2 * ks + 1][2], qa[2 * ks + 1][3])}; PIN(q[ks]); };
#pragma unroll
      for (int n = 8; n < 14; ++n) { rA(0, n); SB(); }
      qa[0] = MS16(A[8], B[1], qa[0]); rA(0, 14);
      qa[1] = MS16(A[9], B[1], qa[1]); rA(0, 15);
      qa[2] = MS16(A[10], B[1], qa[2]);
      qa[3] = MS16(A[11], B[1], qa[3]);
      qa[4] = MS16(A[12], B[1], qa[4]);
      qa[5] = MS16(A[13], B[1], qa[5]);
      pq(0);
      qa[6] = MS16(A[14], B[1], qa[6]);
      qa[7] = MS16(A[15], B[1], qa[7]);
      pq(1);
      sa = MS16(kf[0][0], BC(q[0]), sa); sb = MS16(kf[1][0], BC(q[0]), sb);
      pq(2);
      sa = MS16(kf[0][1], BC(q[1]), sa); sb = MS16(kf[1][1], BC(q[1]), sb);
      pq(3);
      sa = MS16(kf[0][2], BC(q[2]), sa); sb = MS16(kf[1][2], BC(q[2]), sb);
      sa = MS16(kf[0][3], BC(q[3]), sa); sb = MS16(kf[1][3], BC(q[3]), sb);
    }
    CB(10);
    {
      bf16x8 vt[8], wt[16]; f32x4 ot[8], y[4]; v4u of[4];
      auto rT = [&](int n) { vt[n] = trd(a9[n >> 1][0] + 8 * (n & 1), a9[n >> 1][1] + 8 * (n & 1)); };
      auto rN = [&](int n) { const int ks = n >> 2, c = 4 * (ks >> 1) + (n & 3); wt[n] = trd(aq[c][0] + (2 + (ks & 1)) * PG, aq[c][1] + (2 + (ks & 1)) * PG); };
      rT(0); rT(1); rT(2); rT(3); SB();
      float mx, m2;
      asm volatile("s_nop 1\n\tv_max3_f32 %0, %2, %3, %4\n\tv_max3_f32 %1, %5, %6, %7\n\tv_max3_f32 %0, %0, %8, %9\n\tv_max_f32 %0, %0, %1\n\t"
                   "v_mov_b32 %1, %0\n\ts_nop 1\n\tv_permlane16_swap_b32 %0, %1\n\tv_max_f32 %0, %0, %1\n\t"
                   "v_mov_b32 %1, %0\n\ts_nop 1\n\tv_permlane32_swap_b32 %0, %1\n\tv_max_f32 %0, %0, %1"
                   : "=&v"(mx), "=&v"(m2) : "v"(sa[0]), "v"(sa[1]), "v"(sa[2]), "v"(sa[3]), "v"(sb[0]), "v"(sb[1]), "v"(sb[2]), "v"(sb[3]));
      const float c = 0.08838834764831845f * 1.4426950408889634f, mc = -mx * c;
      float e[8], z2;
#pragma unroll
      for (int n = 0; n < 4; ++n) { e[n] = __builtin_amdgcn_exp2f(__builtin_fmaf(sa[n], c, mc)); e[4 + n] = __builtin_amdgcn_exp2f(__builtin_fmaf(sb[n], c, mc)); }
      float z = ((e[0] + e[2]) + (e[4] + e[6])) + ((e[1] + e[3]) + (e[5] + e[7]));
      asm volatile("v_mov_b32 %1, %0\n\ts_nop 1\n\tv_permlane16_swap_b32 %0, %1\n\tv_add_f32 %0, %0, %1\n\t"
                   "v_mov_b32 %1, %0\n\ts_nop 1\n\tv_permlane32_swap_b32 %0, %1\n\tv_add_f32 %0, %0, %1" : "+v"(z), "=&v"(z2));
      const float r = __builtin_amdgcn_rcpf(z);
      const v4u pv = {pk(e[0] * r, e[1] * r), pk(e[2] * r, e[3] * r), pk(e[4] * r, e[5] * r), pk(e[6] * r, e[7] * r)};
      st16(pv, v16, rw, sp0);
      rT(4); rT(5); rT(6); rT(7);
      v2u ck;
#pragma unroll
      for (int n = 0; n < 8; ++n) {
        ot[n] = MS16(vt[n], BC(pv), z4);
        if (n == 0) { ck = row8(fold(pv)); PIN(ck); } else if (n == 1) { ck = half8(ck); ck.x += tag; st8(ck, vcs, rw, sp1); }
        else if (n >= 4) { rV(0, n - 4); rW(n - 4, 14 + ((n - 4) >> 1)); }
      }
      STAMP(61);
      CB(11);
#pragma unroll
      for (int s = 0; s < 4; ++s) {
        rN(2 * s); rN(2 * s + 1);
        of[s] = v4u{pk(ot[2 * s][0], ot[2 * s][1]), pk(ot[2 * s][2], ot[2 * s][3]), pk(ot[2 * s + 1][0], ot[2 * s + 1][1]), pk(ot[2 * s + 1][2], ot[2 * s + 1][3])};
        PIN(of[s]);
        u = MF32(vf[s], bw[s], s ? u : zero);
      }
#pragma unroll
      for (int n = 0; n < 16; ++n) {
        y[n & 3] = MS16(wt[n], BC(of[n >> 2]), n < 4 ? z4 : y[n & 3]);
        if (n < 8) rN(n + 8); else if (n & 1) rW(4 + ((n - 8) >> 1), 9 + ((n - 8) >> 2)); else rV(0, 4 + ((n - 8) >> 1));
      }
      v4u e0, e1; v2u g0, g1;
      CB(12);
      lagW(1, 4, 5, 6, 7, [&](int s) {
        if (s == 0) { e0 = v4u{pk(y[0][0], y[0][1]), pk(y[0][2], y[0][3]), pk(y[1][0], y[1][1]), pk(y[1][2], y[1][3])}; st16(e0, v9, rw, s9a); }
        else if (s == 1) { e1 = v4u{pk(y[2][0], y[2][1]), pk(y[2][2], y[2][3]), pk(y[3][0], y[3][1]), pk(y[3][2], y[3][3])}; st16(e1, v9, rw, s9b); }
        else if (s == 2) { g0 = fold(e0); PIN(g0); } else if (s == 3) { g1 = fold(e1); PIN(g1); }
        else if (s == 4) { g0 = row8(g0); g1 = row8(g1); g0 = col < 8 ? g0 : g1; PIN(g0); }
        else if (s == 5) { g0 = half8(g0); g0.x += tag; st8(g0, vc9, rw, s9c); } });
    }
    CB(13); lagW(2, 11, 0, 1, 8, nop);
    CB(14); lagW(3, 12, 13, 14, 15, nop);
#pragma unroll
    for (int s = 4; s < 8; ++s) u = MF32(vf[s], bw[s], u);
    CB(15);
  }
  {
    const u32 so = ((bp << 5) | (i << 3) | (bb * 4 + (ct >> 1))) * SLAB;
    const u32 sd0 = __builtin_amdgcn_readfirstlane(so + hq * 2048 + (ct & 1) * 1024), sd1 = sd0 + 2 * SLAB;
    const u32 sc0 = __builtin_amdgcn_readfirstlane(so + CMAIN + hq * 128 + (ct & 1) * 32);
    bf16x8 pf[8];
#pragma unroll
    for (int n = 0; n < 8; ++n) { pf[n] = rd16(ap + (n >> 1) * 4096 + (n & 1) * 1024); SB(); }
    uu[3][0] = pack8<0>(u); uu[3][1] = pack8<1>(u);
    f32x16 o = zero;
#pragma unroll
    for (int n = 0; n < 8; ++n) o = MF32(pf[n], BC(uu[n >> 1][n & 1]), o);
    const v4u d0 = pack8<0>(o); st16(d0, v16, rw, sd0);
    const v4u d1 = pack8<1>(o); st16(d1, v16, rw, sd1);
    const v2u f0 = row8(fold(d0)), f1 = row8(fold(d1));
    v2u f = half8(col < 8 ? f0 : f1); f.x += tag; st8(f, vcm, rw, sc0);
  }
  STAMP(58);
  if (w == 7) {
    const u32 vd = v16 + b * SLAB + IN9, vc = row * 8 + b * SLAB + C9;
    v4u d[8]; v2u c[8];
    _Pragma("nounroll") for (int sp = 0; sp < SPIN_CAP; ++sp) {
      asm volatile("buffer_load_dwordx4 %0, %16, %18, 0 offen sc1\n\tbuffer_load_dwordx4 %1, %16, %18, 0 offen offset:1024 sc1\n\t"
                   "buffer_load_dwordx4 %2, %16, %18, 0 offen offset:2048 sc1\n\tbuffer_load_dwordx4 %3, %16, %18, 0 offen offset:3072 sc1\n\t"
                   "buffer_load_dwordx4 %4, %16, %18, %19 offen sc1\n\tbuffer_load_dwordx4 %5, %16, %18, %19 offen offset:1024 sc1\n\t"
                   "buffer_load_dwordx4 %6, %16, %18, %19 offen offset:2048 sc1\n\tbuffer_load_dwordx4 %7, %16, %18, %19 offen offset:3072 sc1\n\t"
                   "buffer_load_dwordx2 %8, %17, %18, 0 offen sc1\n\tbuffer_load_dwordx2 %9, %17, %18, 0 offen offset:32 sc1\n\t"
                   "buffer_load_dwordx2 %10, %17, %18, 0 offen offset:64 sc1\n\tbuffer_load_dwordx2 %11, %17, %18, 0 offen offset:96 sc1\n\t"
                   "buffer_load_dwordx2 %12, %17, %18, 0 offen offset:128 sc1\n\tbuffer_load_dwordx2 %13, %17, %18, 0 offen offset:160 sc1\n\t"
                   "buffer_load_dwordx2 %14, %17, %18, 0 offen offset:192 sc1\n\tbuffer_load_dwordx2 %15, %17, %18, 0 offen offset:224 sc1\n\t"
                   "s_waitcnt vmcnt(0)"
                   : "=&v"(d[0]), "=&v"(d[1]), "=&v"(d[2]), "=&v"(d[3]), "=&v"(d[4]), "=&v"(d[5]), "=&v"(d[6]), "=&v"(d[7]),
                     "=&v"(c[0]), "=&v"(c[1]), "=&v"(c[2]), "=&v"(c[3]), "=&v"(c[4]), "=&v"(c[5]), "=&v"(c[6]), "=&v"(c[7])
                   : "v"(vd), "v"(vc), "s"(rw), "s"(4096) : "memory");
      const v2u f0 = fold(d[3], fold(d[2], fold(d[1], fold(d[0])))), f1 = fold(d[7], fold(d[6], fold(d[5], fold(d[4]))));
      const v2u f = half8(row8(v2u{f0.x + f1.x, f0.y ^ f1.y}));
      u32 cs = 0, cx = 0;
#pragma unroll
      for (int n = 0; n < 8; ++n) { cs += c[n].x; cx ^= c[n].y; }
      if (__all(f.x + 8 * tag == cs && f.y == cx)) break;
      __builtin_amdgcn_s_sleep(2);
    }
    f32x4 y = {0.f, 0.f, 0.f, 0.f};
#pragma unroll
    for (int n = 0; n < 8; ++n) y = MF16(BC(sel), BC(d[n]), y);
    __builtin_amdgcn_raw_buffer_store_b64(v2u{pk(y[0], y[1]), pk(y[2], y[3])}, ry,
                                          row < 2 ? (64 * bp + 16 * (hq >> 1) + col) * 1152 + row * 32 + (512 + 32 * (hq & 1) + 4 * i) * 2 : OOB, 0, 0);
    if (me == 0) __builtin_amdgcn_raw_buffer_store_b32(tag, rw, lane == 0 ? bp * (32 * SLAB) + EPOCH : OOB, 0, 16);
  } else {
    asm volatile("s_waitcnt vmcnt(0)" ::: "memory");
  }
  STAMP(59);
  {
    v4u ga, gb; v2u ca, cb;
    _Pragma("nounroll") for (int sp = 0; sp < SPIN_CAP; ++sp) {
      asm volatile("buffer_load_dwordx4 %0, %4, %7, 0 offen sc1\n\tbuffer_load_dwordx4 %1, %4, %7, %5 offen sc1\n\t"
                   "buffer_load_dwordx2 %2, %6, %7, 0 offen sc1\n\tbuffer_load_dwordx2 %3, %6, %7, 0 offen offset:512 sc1\n\ts_waitcnt vmcnt(0)"
                   : "=&v"(ga), "=&v"(gb), "=&v"(ca), "=&v"(cb) : "v"(vg), "s"(8192), "v"(vgc), "s"(rw) : "memory");
      const v2u f = half8(row8(fold(gb, fold(ga))));
      if (__all(f.x + 2 * tag == ca.x + cb.x && f.y == (ca.y ^ cb.y))) break;
      __builtin_amdgcn_s_sleep(1);
    }
    f32x4 y = {0.f, 0.f, 0.f, 0.f};
    y = MF16(BC(ga), BC(sel), y); y = MF16(BC(gb), BC(sel), y);
    __builtin_amdgcn_raw_buffer_store_b64(v2u{pk(y[0], y[1]), pk(y[2], y[3])}, ry, vy,
                                          4 * ((w >> 1) & 1) * 1152 + (128 * i + 64 * (hq & 1) + 32 * (w >> 2) + 16 * (w & 1)) * 2, 0);
  }
  {

    while (__builtin_amdgcn_s_memrealtime() < dg_t0 + 2500) __builtin_amdgcn_s_sleep(1);
    u64 ta, tb; u32 a_ = lane, b_ = 3;
    asm volatile("s_memrealtime %0\n\ts_waitcnt lgkmcnt(0)" : "=s"(ta) :: "memory");
    if (w < 4) {
    asm volatile("blk_start:\n\t.rept 512\n\tv_add3_u32 %0, %0, %1, %1\n\tv_add3_u32 %1, %1, 1, 1\n\t.endr" : "+v"(a_), "+v"(b_));
    }
    asm volatile("s_memrealtime %0\n\ts_waitcnt lgkmcnt(0)" : "=s"(tb) :: "memory");
    const u64 until = dg_t0 + 4000 + ((w == 0 && blockIdx.x == 77) ? 4 * (tb - ta) : 0); while (__builtin_amdgcn_s_memrealtime() < until && __builtin_amdgcn_s_memrealtime() < dg_t0 + 20000) __builtin_amdgcn_s_sleep(1); }
}

extern "C" void kernel_launch(void* const* d_in, const int* in_sizes, int n_in, void* d_out, int out_size, void* d_ws, size_t ws_size,
                              hipStream_t stream) {
  gqa_fused<<<256, 512, 0, stream>>>(d_in[0], d_in[1], d_in[2], d_in[3], d_in[4], d_out, (char*)d_ws
  );
}
```

```cpp
#include <hip/hip_runtime.h>
#include <hip/hip_bf16.h>

typedef unsigned long long u64;
typedef unsigned u32;
typedef u32 v4u __attribute__((ext_vector_type(4)));
typedef u32 v2u __attribute__((ext_vector_type(2)));
typedef short s16x4 __attribute__((ext_vector_type(4)));
typedef short bf16x8 __attribute__((ext_vector_type(8)));
typedef float f32x16 __attribute__((ext_vector_type(16)));
typedef float f32x4 __attribute__((ext_vector_type(4)));
typedef float f32x2 __attribute__((ext_vector_type(2)));
typedef __bf16 b16x2 __attribute__((ext_vector_type(2)));
#define LAS __attribute__((address_space(3)))
#define GAS __attribute__((address_space(1)))
#define DI __device__ __forceinline__
typedef __amdgpu_buffer_rsrc_t rsrc_t;

constexpr u32 PG = 8192;
constexpr u32 SLAB = 32768;
constexpr u32 IN9 = 16384, POUT = 24576, CMAIN = 28672, C9 = 29696, CP = 30208, EPOCH = 31744;
constexpr u32 WS_BYTES = 256 * SLAB;
constexpr u32 OOB = 0x80000000u;
constexpr int SPIN_CAP = 1 << 16;
#define CODE_BYTES 17920
#define PACE 6
#define PSLEEP 1
#define KPOS 0
#define SH0 1
#define SH1 2
#define SH2 3
#define SH3 4
#define HPRIO 2
#define PPOS 3
#define GSLEEP 20

#define STAMP(i)
#define SB() __builtin_amdgcn_sched_barrier(0)
#define HT(k, N) asm volatile("s_waitcnt vmcnt(%0) lgkmcnt(0)\n\ts_barrier" :: "i"(N) : "memory")
#define CT(k) do { SB(); asm volatile("s_waitcnt lgkmcnt(0)\n\ts_barrier" ::: "memory"); SB(); } while (0)
#define CB(k) do { SB(); asm volatile("s_barrier" ::: "memory"); SB(); } while (0)
#define HV(k, N) asm volatile("s_waitcnt vmcnt(%0)\n\ts_barrier" :: "i"(N) : "memory")
#define WV(N) asm volatile("s_waitcnt vmcnt(%0)" :: "i"(N) : "memory")
#define PIN(v) asm volatile("" : "+v"(v))

template <bool NT> DI void dma(u32 lds, u32 voff, rsrc_t rs, u32 soff) {
  if constexpr (NT) asm volatile("s_mov_b32 m0, %0\n\ts_nop 0\n\tbuffer_load_dwordx4 %1, %2, %3 offen lds" :: "s"(lds), "v"(voff), "s"(rs), "s"(soff) : "memory");
  else              asm volatile("s_mov_b32 m0, %0\n\ts_nop 0\n\tbuffer_load_dwordx4 %1, %2, %3 offen lds" :: "s"(lds), "v"(voff), "s"(rs), "s"(soff) : "memory");
}
DI void st16(v4u v, u32 voff, rsrc_t rs, u32 soff) {
  asm volatile("buffer_store_dwordx4 %0, %1, %2, %3 offen sc1\n\ts_nop 1" :: "v"(v), "v"(voff), "s"(rs), "s"(soff) : "memory");
}
DI void st8(v2u v, u32 voff, rsrc_t rs, u32 soff) {
  asm volatile("buffer_store_dwordx2 %0, %1, %2, %3 offen sc1" :: "v"(v), "v"(voff), "s"(rs), "s"(soff) : "memory");
}
DI s16x4 tr4(u32 a) { return __builtin_amdgcn_ds_read_tr16_b64_v4i16((LAS s16x4*)(size_t)a); }
DI bf16x8 trd(u32 a0, u32 a1) { return __builtin_shufflevector(tr4(a0), tr4(a1), 0, 1, 2, 3, 4, 5, 6, 7); }
DI bf16x8 rd16(u32 a) { return *(LAS bf16x8*)(size_t)a; }
DI u32 pk(float a, float b) { f32x2 v = {a, b}; return __builtin_bit_cast(u32, __builtin_convertvector(v, b16x2)); }
template <int S> DI v4u pack8(const f32x16& c) {
  v4u r = {pk(c[8 * S], c[8 * S + 1]), pk(c[8 * S + 2], c[8 * S + 3]), pk(c[8 * S + 4], c[8 * S + 5]), pk(c[8 * S + 6], c[8 * S + 7])};
  PIN(r);
  return r;
}
DI f32x16 MF32(bf16x8 a, bf16x8 b, f32x16 c) { SB(); f32x16 d = __builtin_amdgcn_mfma_f32_32x32x16_bf16(a, b, c, 0, 0, 0); PIN(d); SB(); return d; }
DI f32x4 MS16(bf16x8 a, bf16x8 b, f32x4 c) { SB(); f32x4 d = __builtin_amdgcn_mfma_f32_16x16x32_bf16(a, b, c, 0, 0, 0); PIN(d); SB(); return d; }
#define MF16(a, b, c) __builtin_amdgcn_mfma_f32_16x16x32_bf16((a), (b), (c), 0, 0, 0)
#define BC(v) __builtin_bit_cast(bf16x8, (v))
template <int C> DI float dppf(float v) { return __builtin_bit_cast(float, __builtin_amdgcn_mov_dpp(__builtin_bit_cast(int, v), C, 0xF, 0xF, true)); }
template <int C> DI u32 dppu(u32 v) { return (u32)__builtin_amdgcn_mov_dpp((int)v, C, 0xF, 0xF, true); }
DI u32 xor3(u32 a, u32 b, u32 c) { return __builtin_amdgcn_bitop3_b32(a, b, c, 0x96); }
DI v2u fold(v4u d) { return v2u{(d.x + d.y + d.z) + d.w, xor3(d.x, d.y, d.z) ^ d.w}; }
DI v2u fold(v4u d, v2u a) { return v2u{(d.x + d.y + a.x) + d.z + d.w, xor3(xor3(d.x, d.y, a.y), d.z, d.w)}; }
DI v2u row8(v2u v) { return v2u{v.x + dppu<0x128>(v.x), v.y ^ dppu<0x128>(v.y)}; }
DI v2u half8(v2u v) {
  v.x += dppu<0x141>(v.x); v.y ^= dppu<0x141>(v.y); v.x += dppu<0x4E>(v.x); v.y ^= dppu<0x4E>(v.y);
  v.x += dppu<0xB1>(v.x); v.y ^= dppu<0xB1>(v.y);
  return v;
}

__global__ void __launch_bounds__(512, 2)
gqa_fused(const void* __restrict__ x, const void* __restrict__ kc, const void* __restrict__ vc, const void* __restrict__ wq,
          const void* __restrict__ wo, void* __restrict__ out, char* __restrict__ ws
) {
  __shared__ __attribute__((aligned(1024))) char smem[20 * PG];
  u64 pc0;
  asm volatile("s_getpc_b64 %0" : "=s"(pc0));
  const u32 L = (u32)(size_t)(LAS char*)smem;
  const u32 tid = threadIdx.x, lane = tid & 63;
  const u32 w = __builtin_amdgcn_readfirstlane(tid >> 6);
  const u32 b = blockIdx.x, hq = b & 7, bp = b >> 5, i = (b >> 3) & 3, h = 4 * hq + i;
  STAMP(0);
  const rsrc_t rw = __builtin_amdgcn_make_buffer_rsrc((void*)ws, 0, WS_BYTES, 0x00020000);
  const rsrc_t ry = __builtin_amdgcn_make_buffer_rsrc(out, 0, 512 * 1152, 0x00020000);
  const u32 me = hq * 4 + i, row = lane >> 4, col = lane & 15;
  const u32 hh = lane >> 5, l31 = lane & 31;
  const u32 ct = w & 3, bb = w >> 2, q4 = col >> 2, p4 = lane & 3;
  f32x16 zero;
#pragma unroll
  for (int n = 0; n < 16; ++n) zero[n] = 0.f;
  u32 tag, vy, vg, vgc, v16, vcm, ap, aw[2][2], av[8]; v4u sel;
  bf16x8 vf[8], bw[8]; v4u uu[4][2]; f32x16 u;
  auto setup2 = [&]() {
    const u32 one = (col & 1) ? 0x3F800000u : 0x00003F80u;
    sel = v4u{(col >> 1) == 0 ? one : 0u, (col >> 1) == 1 ? one : 0u, (col >> 1) == 2 ? one : 0u, (col >> 1) == 3 ? one : 0u};
    const u32 yrow = 64 * bp + 32 * (hq >> 2) + 16 * ((hq >> 1) & 1) + (col & 3) + 8 * ((col >> 2) & 1);
    vy = col < 8 ? yrow * 1152 + 8 * row : OOB;
    vg = row * 2048 + w * 256 + col * 16 + b * SLAB; vgc = row * 128 + w * 8 + b * SLAB + CMAIN;
    v16 = lane * 16; vcm = (col & 7) == 0 ? (col >> 3) * (2 * SLAB) + row * 8 : OOB;
    ap = L + 2 * PG + bb * 2048 + lane * 16;
    PIN(sel); PIN(vy); PIN(vg); PIN(vgc); PIN(v16); PIN(vcm); PIN(ap);
#pragma unroll
    for (int t = 0; t < 2; ++t) {
      aw[0][t] = L + 256 * (8 * hh + 4 * t + q4) + 16 * ((4 * ct + 2 * (row & 1) + (p4 >> 1)) ^ ((q4 << 2) | (2 * hh + t))) + 8 * (p4 & 1);
      aw[1][t] = aw[0][t] + 8 * PG;
      PIN(aw[0][t]); PIN(aw[1][t]);
    }
    const u32 arow = 256 * l31 + 16 * (hh ^ (((l31 & 3) << 2) | ((l31 >> 2) & 3)));
#pragma unroll
    for (int n = 0; n < 8; ++n) { av[n] = L + (16 + bb) * PG + (arow ^ (128 * (bp & 1)) ^ (32 * n)); PIN(av[n]); }
  };
  auto rV = [&](int slot, int s) { vf[s] = rd16(av[s] + 2 * slot * PG); };
  auto rW = [&](int s, int pg) { const u32 o = (pg & 7) * PG + (s & 1) * 4096; bw[s] = trd(aw[pg >> 3][0] + o, aw[pg >> 3][1] + o); };
  auto headW = [&](int hs, int p0, int p1, int p2, int p3, auto f) {
    const int pg[4] = {p0, p1, p2, p3};
    const f32x16 o = u;
    rW(0, p0); SB(); rW(1, p0); SB(); rW(2, p1); SB(); rW(3, p1); SB();
    uu[hs - 1][0] = pack8<0>(o); uu[hs - 1][1] = pack8<1>(o);
#pragma unroll
    for (int s = 0; s < 8; ++s) {
      u = MF32(vf[s], bw[s], s ? u : zero);
      if (s < 4) rW(s + 4, pg[2 + (s >> 1)]);
      if (hs == 1) rV(1, s);
      f(s);
    }
  };
  auto lagW = [&](int hs, int p0, int p1, int p2, int p3, auto f) {
    const int pg[4] = {p0, p1, p2, p3};
#pragma unroll
    for (int s = 4; s < 8; ++s) { u = MF32(vf[s], bw[s], u); rW(s - 4, pg[(s - 4) >> 1]); if (hs == 2) rV(1, s); f(s - 4); }
    const f32x16 o = u;
#pragma unroll
    for (int s = 0; s < 4; ++s) {
      u = MF32(vf[s], bw[s], s ? u : zero);
      rW(s + 4, pg[2 + (s >> 1)]);
      if (hs == 1) rV(1, s);
      if (s == 2) uu[hs - 1][0] = pack8<0>(o);
      if (s == 3) uu[hs - 1][1] = pack8<1>(o);
      f(s + 4);
    }
  };
  auto nop = [](int) {};
  asm volatile("" :: "s"(x), "s"(kc), "s"(vc), "s"(wq), "s"(wo), "s"(out), "s"(ws));

  if (__builtin_expect(w >= 4, 1)) {
    const u32 g = w - 4;
    const rsrc_t rx = __builtin_amdgcn_make_buffer_rsrc((void*)x, 0, 512 * 1152, 0x00020000);
    const rsrc_t rk = __builtin_amdgcn_make_buffer_rsrc((void*)kc, 0, 256 * 8192, 0x00020000);
    const rsrc_t rv = __builtin_amdgcn_make_buffer_rsrc((void*)vc, 0, 256 * 8192, 0x00020000);
    const rsrc_t rq = __builtin_amdgcn_make_buffer_rsrc((void*)wq, 0, 576 * 8192, 0x00020000);
    const rsrc_t ro = __builtin_amdgcn_make_buffer_rsrc((void*)wo, 0, 4096 * 1152, 0x00020000);
    const u32 xr = 8 * g + (lane >> 3);
    const u32 vx = xr * 1152 + 16 * ((lane & 7) ^ ((xr >> 1) & 7));
    const u32 pr = 4 * g + row;
    const u32 pc = col ^ ((row << 2) | g);
    const u32 vq = pr * 8192 + 16 * pc;
    const u32 Lg = L + 1024 * g;
    const u32 r0 = 3 * ((bp + i) % 3);
    const u32 sx = bp * (64 * 1152), sq = 256 * h;
    auto step = [&](int s) {
      u32 ks = r0 + s; ks = ks >= 9 ? ks - 9 : ks;
      const u32 o = sx + ks * 128, o2 = sq + ks * (64 * 8192);
      dma<false>(Lg + (8 + (s & 3)) * PG, vx, rx, o); dma<false>(Lg + (8 + (s & 3)) * PG + 4096, vx, rx, o + 32 * 1152);
#pragma unroll
      for (int n = 0; n < 4; ++n) dma<true>(Lg + 2 * (s & 3) * PG + n * 4096, vq, rq, o2 + n * (16 * 8192));
    };
    step(0); step(1);
    {
      const rsrc_t rc = __builtin_amdgcn_make_buffer_rsrc((void*)(pc0 & ~255ull), 0, CODE_BYTES, 0x00020000);
      asm volatile("s_buffer_load_dword s100, %0, %1\n\ts_buffer_load_dword s101, %2, %3\n\ts_buffer_load_dword s101, %2, %3 offset:64\n\t"
                   "s_buffer_load_dword s101, %2, %3 offset:128\n\ts_buffer_load_dword s101, %4, %5"
                   :: "s"(rw), "s"(bp * (32 * SLAB) + EPOCH), "s"(rc), "s"(((b >> 3) * 4 + g) * 192), "s"(ry), "s"(bp * (64 * 1152)) : "s100", "s101", "memory");
    }
    u32 vkv = pr * 256 + 16 * pc, vo = pr * 1152 + 16 * pc, vo9 = (pr + 64 * (pc >> 3)) * 1152 + 16 * (pc & 7);
    u32 pl = L + 2 * PG + g * 4096 + row * 512 + col * 16;
    u32 vcl = row * 8;
    PIN(vkv); PIN(vo); PIN(vo9); PIN(pl); PIN(vcl);
    u32 bs = b;
    asm volatile("" : "+s"(bs));
    setup2();
    auto ldk = [&](u32 n) {
      const u32 o = ((2 * bp + n) * 16 + (h >> 1)) * 8192;
      dma<true>(Lg + (12 + n) * PG, vkv, rk, o); dma<true>(Lg + (12 + n) * PG + 4096, vkv, rk, o + 4096);
    };
    auto ldv = [&](u32 n) {
      const u32 o = ((2 * bp + (n & 1)) * 16 + 2 * hq + ((n >> 1) ^ (bp >> 2))) * 8192;
      dma<true>(Lg + (16 + n) * PG, vkv, rv, o); dma<true>(Lg + (16 + n) * PG + 4096, vkv, rv, o + 4096);
    };
    auto ldw1 = [&](int n, int pg, int k) {
      dma<true>(Lg + pg * PG + k * 4096, vo, ro, ((bs & 7) * 512 + 32 * (n ^ ((bs >> 5) << 1)) + 16 * k) * 1152 + 256 * ((bs >> 3) & 3));
    };
    auto ldw = [&](int n, int pg) { ldw1(n, pg, 0); ldw1(n, pg, 1); };
    auto ldn = [&](int n, int pg) {
      const u32 o = (h * 128 + 32 * n) * 1152 + 1024;
      dma<true>(Lg + pg * PG, vo9, ro, o); dma<true>(Lg + pg * PG + 4096, vo9, ro, o + 16 * 1152);
    };
    auto extra = [&](int t) {
      if (t == KPOS) { ldk(i & 1); ldk((i & 1) ^ 1); }
      const u32 grp = bp & 3;
      if ((t == SH0 && grp == 0) || (t == SH1 && grp == 1) || (t == SH2 && grp == 2) || (t == SH3 && grp == 3)) {
        ldv(i); ldw(0, 14); ldw(1, 15);
        dma<true>(Lg + (16 + (i ^ 1)) * PG, vo9, ro, (h * 128 + 16 * (bp & 3)) * 1152 + 1024);
      }
    };
    step(2);
    HV(0, 12); step(3); extra(0);
    HV(1, 12); step(4); extra(1);
    HV(2, 12); step(5); extra(2);
    HV(3, 12); step(6); extra(3);
    HV(4, 12); step(7); extra(4);
    HV(5, 12); step(8);
    HV(6, 12); ldv(i ^ 1); ldv(i ^ 2);
    HV(7, 10); ldv(i ^ 3);
    asm volatile("s_waitcnt lgkmcnt(0)\n\ts_mov_b32 %0, s100" : "=s"(tag) :: "memory");
    tag += 1;
    HV(8, 8);  ldw(2, 9);
    HV(9, 8);  WV(PACE); ldw(3, 10); WV(PACE); ldn(0, 2); WV(PACE); ldn(1, 3);
    HV(10, 8);
    rV(0, 0); rW(0, 14); SB(); rV(0, 1); rW(1, 14); SB(); rV(0, 2); rW(2, 15); SB(); rV(0, 3); rW(3, 15);
#pragma unroll
    for (int s = 0; s < 4; ++s) { u = MF32(vf[s], bw[s], s ? u : zero); rV(0, s + 4); }
    WV(PACE); ldw(4, 4); WV(PACE); ldw(5, 5); WV(PACE); ldw(6, 6); WV(PACE); ldw(7, 7);
    HV(11, 8);
    rW(4, 9); SB(); rW(5, 9); SB(); rW(6, 10); SB(); rW(7, 10); SB();
    ldw(8, 11);
#pragma unroll
    for (int s = 4; s < 8; ++s) u = MF32(vf[s], bw[s], u);
    WV(PACE); ldw(9, 0); WV(PACE); ldw(10, 1); WV(PACE); ldw(11, 8);
    __builtin_amdgcn_s_setprio(HPRIO);
    HV(12, 8);
    headW(1, 4, 5, 6, 7, [&](int s) { ldw1(12 + (s >> 1), 12 + (s >> 1), s & 1); });
    HV(13, 8);
    v4u p0, p1, p2, p3; v2u c0, c1, c2, c3;
    const u32 spl = ((bp << 5) | ((g ^ (bp >> 1)) << 3) | hq) * SLAB;
#define LOAD_P "buffer_load_dwordx4 %0, %8, %10, %11 offen sc1\n\tbuffer_load_dwordx4 %1, %8, %10, %11 offen offset:1024 sc1\n\t" \
               "buffer_load_dwordx4 %2, %8, %10, %11 offen offset:2048 sc1\n\tbuffer_load_dwordx4 %3, %8, %10, %11 offen offset:3072 sc1\n\t" \
               "buffer_load_dwordx2 %4, %9, %10, %12 offen sc1\n\tbuffer_load_dwordx2 %5, %9, %10, %12 offen offset:32 sc1\n\t" \
               "buffer_load_dwordx2 %6, %9, %10, %12 offen offset:64 sc1\n\tbuffer_load_dwordx2 %7, %9, %10, %12 offen offset:96 sc1"
#define P_ARGS "=&v"(p0), "=&v"(p1), "=&v"(p2), "=&v"(p3), "=&v"(c0), "=&v"(c1), "=&v"(c2), "=&v"(c3) : "v"(v16), "v"(vcl), "s"(rw), "s"(spl + POUT), "s"(spl + CP) : "memory"
    headW(2, 11, 0, 1, 8, [&](int s) { if (s == PPOS) asm volatile(LOAD_P : P_ARGS); });
    HV(14, 8);
    headW(3, 12, 13, 14, 15, nop);
    asm volatile("s_waitcnt vmcnt(0)" : "+v"(p0), "+v"(p1), "+v"(p2), "+v"(p3), "+v"(c0), "+v"(c1), "+v"(c2), "+v"(c3) :: "memory");
    STAMP(62);
    _Pragma("nounroll") for (int sp = 0; sp < SPIN_CAP; ++sp) {
      *(LAS v4u*)(size_t)pl = p0; *(LAS v4u*)(size_t)(pl + 256) = p1; *(LAS v4u*)(size_t)(pl + 2048) = p2; *(LAS v4u*)(size_t)(pl + 2304) = p3;
      const v2u f0 = fold(p1, fold(p0)), f1 = fold(p3, fold(p2));
      const v2u f = half8(row8(v2u{f0.x + f1.x, f0.y ^ f1.y}));
      if (__all(f.x + 4 * tag == (c0.x + c1.x) + (c2.x + c3.x) && f.y == (xor3(c0.y, c1.y, c2.y) ^ c3.y))) break;
      asm volatile("s_waitcnt lgkmcnt(0)\n\t" LOAD_P "\n\ts_waitcnt vmcnt(0)" : P_ARGS);
    }
    HT(15, 0);
  } else {
    setup2();
    const u32 mt = w >> 1;
    u32 aq[8][2], ax[2], ak[8], a9[4][2];
    const u32 xrow = 16 * w + col, key = 16 * (col >> 3) + (col & 7);
    ax[0] = L + 8 * PG + 128 * xrow + 16 * (row ^ ((xrow >> 1) & 7)); ax[1] = ax[0] ^ 64;
    PIN(ax[0]); PIN(ax[1]);
#pragma unroll
    for (int n = 0; n < 8; ++n) {
#pragma unroll
      for (int t = 0; t < 2; ++t) {
        aq[n][t] = L + 256 * (8 * row + 4 * t + q4) + 16 * ((2 * n + (p4 >> 1)) ^ ((q4 << 2) | ((2 * row + t) & 3))) + 8 * (p4 & 1);
        PIN(aq[n][t]);
      }
      ak[n] = L + (12 + mt) * PG + 256 * key + 16 * ((2 * n + (row >> 1)) ^ (((key & 3) << 2) | ((key >> 2) & 3))) + 8 * (row & 1);
      a9[n >> 1][n & 1] = L + (16 + mt + 2 * ((i >> 1) ^ (bp >> 2))) * PG + 256 * (16 * (row >> 1) + 4 * (row & 1) + 8 * (n & 1) + q4) +
                          16 * ((4 * (n >> 1) + p4) ^ ((q4 << 2) | ((row & 1) + 2 * (n & 1))));
      PIN(ak[n]); PIN(a9[n >> 1][n & 1]);
    }
    u32 v9 = (row << 3) * SLAB + col * 16, vc9 = (col & 7) == 0 ? (row << 3) * SLAB + (col >> 3) * SLAB : OOB, vcs = col == 0 ? row * 8 : OOB;
    PIN(v9); PIN(vc9); PIN(vcs);
    const u32 sp0 = b * SLAB + POUT + w * 1024, sp1 = b * SLAB + CP + w * 32;
    const u32 s9 = ((bp << 5) | (2 * w)) * SLAB;
    const u32 s9a = s9 + IN9 + me * 256, s9b = s9a + SLAB, s9c = s9 + C9 + me * 8;
    asm volatile("" :: "s"(sp0), "s"(sp1), "s"(s9a), "s"(s9b), "s"(s9c));

    const f32x4 z4 = {0.f, 0.f, 0.f, 0.f};
    f32x4 qa[8];
#pragma unroll
    for (int n = 0; n < 8; ++n) qa[n] = z4;
    bf16x8 A[16], B[2], kf[2][4];
    auto rA = [&](int sl, int n) { const u32 o = (2 * sl + (n >> 3)) * PG; A[n] = trd(aq[n & 7][0] + o, aq[n & 7][1] + o); };
    auto rB = [&](int sl, int k) { B[k] = rd16(ax[k] + sl * PG); };
    auto rK = [&](int T, int ks) {
      const s16x4 lo = *(LAS s16x4*)(size_t)(ak[2 * ks + T] + T * 2048), hi = *(LAS s16x4*)(size_t)(ak[2 * ks + 1 - T] + T * 2048);
      kf[T][ks] = __builtin_shufflevector(lo, hi, 0, 1, 2, 3, 4, 5, 6, 7);
    };
    bf16x8 Bc;
    auto head = [&](int sl, bool carry) {
      rB(sl, 0);
#pragma unroll
      for (int n = 0; n < 6; ++n) { rA(sl, n); if (carry) qa[n + 2] = MS16(A[n + 10], Bc, qa[n + 2]); else SB(); }
      rB(sl, 1);
    };
    auto p1 = [&](int t) {
      head(t & 3, t > 0);
#pragma unroll
      for (int n = 0; n < 10; ++n) { qa[n & 7] = MS16(A[n], B[n >> 3], qa[n & 7]); rA(t & 3, n + 6); }
      Bc = B[1];
      if (t == KPOS + 4) { rK(0, 0); rK(0, 1); rK(0, 2); rK(0, 3); }
      if (t == KPOS + 5) { rK(1, 0); rK(1, 1); rK(1, 2); rK(1, 3); }
    };
    CT(0); p1(0); CT(1); p1(1); CT(2); p1(2); CT(3); p1(3);
    asm volatile("s_buffer_load_dword %0, %1, %2\n\ts_waitcnt lgkmcnt(0)" : "=s"(tag) : "s"(rw), "s"(bp * (32 * SLAB) + EPOCH) : "memory");
    tag += 1;
    CT(4); p1(4); CT(5); p1(5); CB(6); p1(6); CB(7); p1(7);
    CB(8);
    head(0, true);
#pragma unroll
    for (int n = 0; n < 8; ++n) { qa[n] = MS16(A[n], B[0], qa[n]); if (n < 2) rA(0, n + 6); }
    CB(9);
    f32x4 sa = z4, sb = z4;
    {
      v4u q[4];
      auto pq = [&](int ks) { q[ks] = v4u{pk(qa[2 * ks][0], qa[2 * ks][1]), pk(qa[2 * ks][2], qa[2 * ks][3]), pk(qa[2 * ks + 1][0], qa[2 * ks + 1][1]), pk(qa[2 * ks + 1][2], qa[2 * ks + 1][3])}; PIN(q[ks]); };
#pragma unroll
      for (int n = 8; n < 14; ++n) { rA(0, n); SB(); }
      qa[0] = MS16(A[8], B[1], qa[0]); rA(0, 14);
      qa[1] = MS16(A[9], B[1], qa[1]); rA(0, 15);
      qa[2] = MS16(A[10], B[1], qa[2]);
      qa[3] = MS16(A[11], B[1], qa[3]);
      qa[4] = MS16(A[12], B[1], qa[4]);
      qa[5] = MS16(A[13], B[1], qa[5]);
      pq(0);
      qa[6] = MS16(A[14], B[1], qa[6]);
      qa[7] = MS16(A[15], B[1], qa[7]);
      pq(1);
      sa = MS16(kf[0][0], BC(q[0]), sa); sb = MS16(kf[1][0], BC(q[0]), sb);
      pq(2);
      sa = MS16(kf[0][1], BC(q[1]), sa); sb = MS16(kf[1][1], BC(q[1]), sb);
      pq(3);
      sa = MS16(kf[0][2], BC(q[2]), sa); sb = MS16(kf[1][2], BC(q[2]), sb);
      sa = MS16(kf[0][3], BC(q[3]), sa); sb = MS16(kf[1][3], BC(q[3]), sb);
    }
    CB(10);
    {
      bf16x8 vt[8], wt[16]; f32x4 ot[8], y[4]; v4u of[4];
      auto rT = [&](int n) { vt[n] = trd(a9[n >> 1][0] + 8 * (n & 1), a9[n >> 1][1] + 8 * (n & 1)); };
      auto rN = [&](int n) { const int ks = n >> 2, c = 4 * (ks >> 1) + (n & 3); wt[n] = trd(aq[c][0] + (2 + (ks & 1)) * PG, aq[c][1] + (2 + (ks & 1)) * PG); };
      rT(0); rT(1); rT(2); rT(3); SB();
      float mx, m2;
      asm volatile("s_nop 1\n\tv_max3_f32 %0, %2, %3, %4\n\tv_max3_f32 %1, %5, %6, %7\n\tv_max3_f32 %0, %0, %8, %9\n\tv_max_f32 %0, %0, %1\n\t"
                   "v_mov_b32 %1, %0\n\ts_nop 1\n\tv_permlane16_swap_b32 %0, %1\n\tv_max_f32 %0, %0, %1\n\t"
                   "v_mov_b32 %1, %0\n\ts_nop 1\n\tv_permlane32_swap_b32 %0, %1\n\tv_max_f32 %0, %0, %1"
                   : "=&v"(mx), "=&v"(m2) : "v"(sa[0]), "v"(sa[1]), "v"(sa[2]), "v"(sa[3]), "v"(sb[0]), "v"(sb[1]), "v"(sb[2]), "v"(sb[3]));
      const float c = 0.08838834764831845f * 1.4426950408889634f, mc = -mx * c;
      float e[8], z2;
#pragma unroll
      for (int n = 0; n < 4; ++n) { e[n] = __builtin_amdgcn_exp2f(__builtin_fmaf(sa[n], c, mc)); e[4 + n] = __builtin_amdgcn_exp2f(__builtin_fmaf(sb[n], c, mc)); }
      float z = ((e[0] + e[2]) + (e[4] + e[6])) + ((e[1] + e[3]) + (e[5] + e[7]));
      asm volatile("v_mov_b32 %1, %0\n\ts_nop 1\n\tv_permlane16_swap_b32 %0, %1\n\tv_add_f32 %0, %0, %1\n\t"
                   "v_mov_b32 %1, %0\n\ts_nop 1\n\tv_permlane32_swap_b32 %0, %1\n\tv_add_f32 %0, %0, %1" : "+v"(z), "=&v"(z2));
      const float r = __builtin_amdgcn_rcpf(z);
      const v4u pv = {pk(e[0] * r, e[1] * r), pk(e[2] * r, e[3] * r), pk(e[4] * r, e[5] * r), pk(e[6] * r, e[7] * r)};
      st16(pv, v16, rw, sp0);
      rT(4); rT(5); rT(6); rT(7);
      v2u ck;
#pragma unroll
      for (int n = 0; n < 8; ++n) {
        ot[n] = MS16(vt[n], BC(pv), z4);
        if (n == 0) { ck = row8(fold(pv)); PIN(ck); } else if (n == 1) { ck = half8(ck); ck.x += tag; st8(ck, vcs, rw, sp1); }
        else if (n >= 4) { rV(0, n - 4); rW(n - 4, 14 + ((n - 4) >> 1)); }
      }
      STAMP(61);
      CB(11);
#pragma unroll
      for (int s = 0; s < 4; ++s) {
        rN(2 * s); rN(2 * s + 1);
        of[s] = v4u{pk(ot[2 * s][0], ot[2 * s][1]), pk(ot[2 * s][2], ot[2 * s][3]), pk(ot[2 * s + 1][0], ot[2 * s + 1][1]), pk(ot[2 * s + 1][2], ot[2 * s + 1][3])};
        PIN(of[s]);
        u = MF32(vf[s], bw[s], s ? u : zero);
      }
#pragma unroll
      for (int n = 0; n < 16; ++n) {
        y[n & 3] = MS16(wt[n], BC(of[n >> 2]), n < 4 ? z4 : y[n & 3]);
        if (n < 8) rN(n + 8); else if (n & 1) rW(4 + ((n - 8) >> 1), 9 + ((n - 8) >> 2)); else rV(0, 4 + ((n - 8) >> 1));
      }
      v4u e0, e1; v2u g0, g1;
      CB(12);
      lagW(1, 4, 5, 6, 7, [&](int s) {
        if (s == 0) { e0 = v4u{pk(y[0][0], y[0][1]), pk(y[0][2], y[0][3]), pk(y[1][0], y[1][1]), pk(y[1][2], y[1][3])}; st16(e0, v9, rw, s9a); }
        else if (s == 1) { e1 = v4u{pk(y[2][0], y[2][1]), pk(y[2][2], y[2][3]), pk(y[3][0], y[3][1]), pk(y[3][2], y[3][3])}; st16(e1, v9, rw, s9b); }
        else if (s == 2) { g0 = fold(e0); PIN(g0); } else if (s == 3) { g1 = fold(e1); PIN(g1); }
        else if (s == 4) { g0 = row8(g0); g1 = row8(g1); g0 = col < 8 ? g0 : g1; PIN(g0); }
        else if (s == 5) { g0 = half8(g0); g0.x += tag; st8(g0, vc9, rw, s9c); } });
    }
    CB(13); lagW(2, 11, 0, 1, 8, nop);
    CB(14); lagW(3, 12, 13, 14, 15, nop);
#pragma unroll
    for (int s = 4; s < 8; ++s) u = MF32(vf[s], bw[s], u);
    CB(15);
  }
  {
    const u32 so = ((bp << 5) | (i << 3) | (bb * 4 + (ct >> 1))) * SLAB;
    const u32 sd0 = __builtin_amdgcn_readfirstlane(so + hq * 2048 + (ct & 1) * 1024), sd1 = sd0 + 2 * SLAB;
    const u32 sc0 = __builtin_amdgcn_readfirstlane(so + CMAIN + hq * 128 + (ct & 1) * 32);
    bf16x8 pf[8];
#pragma unroll
    for (int n = 0; n < 8; ++n) { pf[n] = rd16(ap + (n >> 1) * 4096 + (n & 1) * 1024); SB(); }
    uu[3][0] = pack8<0>(u); uu[3][1] = pack8<1>(u);
    f32x16 o = zero;
#pragma unroll
    for (int n = 0; n < 8; ++n) o = MF32(pf[n], BC(uu[n >> 1][n & 1]), o);
    const v4u d0 = pack8<0>(o); st16(d0, v16, rw, sd0);
    const v4u d1 = pack8<1>(o); st16(d1, v16, rw, sd1);
    const v2u f0 = row8(fold(d0)), f1 = row8(fold(d1));
    v2u f = half8(col < 8 ? f0 : f1); f.x += tag; st8(f, vcm, rw, sc0);
  }
  STAMP(58);
  if (w == 7) {
    const u32 vd = v16 + b * SLAB + IN9, vc = row * 8 + b * SLAB + C9;
    v4u d[8]; v2u c[8];
    _Pragma("nounroll") for (int sp = 0; sp < SPIN_CAP; ++sp) {
      asm volatile("buffer_load_dwordx4 %0, %16, %18, 0 offen sc1\n\tbuffer_load_dwordx4 %1, %16, %18, 0 offen offset:1024 sc1\n\t"
                   "buffer_load_dwordx4 %2, %16, %18, 0 offen offset:2048 sc1\n\tbuffer_load_dwordx4 %3, %16, %18, 0 offen offset:3072 sc1\n\t"
                   "buffer_load_dwordx4 %4, %16, %18, %19 offen sc1\n\tbuffer_load_dwordx4 %5, %16, %18, %19 offen offset:1024 sc1\n\t"
                   "buffer_load_dwordx4 %6, %16, %18, %19 offen offset:2048 sc1\n\tbuffer_load_dwordx4 %7, %16, %18, %19 offen offset:3072 sc1\n\t"
                   "buffer_load_dwordx2 %8, %17, %18, 0 offen sc1\n\tbuffer_load_dwordx2 %9, %17, %18, 0 offen offset:32 sc1\n\t"
                   "buffer_load_dwordx2 %10, %17, %18, 0 offen offset:64 sc1\n\tbuffer_load_dwordx2 %11, %17, %18, 0 offen offset:96 sc1\n\t"
                   "buffer_load_dwordx2 %12, %17, %18, 0 offen offset:128 sc1\n\tbuffer_load_dwordx2 %13, %17, %18, 0 offen offset:160 sc1\n\t"
                   "buffer_load_dwordx2 %14, %17, %18, 0 offen offset:192 sc1\n\tbuffer_load_dwordx2 %15, %17, %18, 0 offen offset:224 sc1\n\t"
                   "s_waitcnt vmcnt(0)"
                   : "=&v"(d[0]), "=&v"(d[1]), "=&v"(d[2]), "=&v"(d[3]), "=&v"(d[4]), "=&v"(d[5]), "=&v"(d[6]), "=&v"(d[7]),
                     "=&v"(c[0]), "=&v"(c[1]), "=&v"(c[2]), "=&v"(c[3]), "=&v"(c[4]), "=&v"(c[5]), "=&v"(c[6]), "=&v"(c[7])
                   : "v"(vd), "v"(vc), "s"(rw), "s"(4096) : "memory");
      const v2u f0 = fold(d[3], fold(d[2], fold(d[1], fold(d[0])))), f1 = fold(d[7], fold(d[6], fold(d[5], fold(d[4]))));
      const v2u f = half8(row8(v2u{f0.x + f1.x, f0.y ^ f1.y}));
      u32 cs = 0, cx = 0;
#pragma unroll
      for (int n = 0; n < 8; ++n) { cs += c[n].x; cx ^= c[n].y; }
      if (__all(f.x + 8 * tag == cs && f.y == cx)) break;
      __builtin_amdgcn_s_sleep(2);
    }
    f32x4 y = {0.f, 0.f, 0.f, 0.f};
#pragma unroll
    for (int n = 0; n < 8; ++n) y = MF16(BC(sel), BC(d[n]), y);
    __builtin_amdgcn_raw_buffer_store_b64(v2u{pk(y[0], y[1]), pk(y[2], y[3])}, ry,
                                          row < 2 ? (64 * bp + 16 * (hq >> 1) + col) * 1152 + row * 32 + (512 + 32 * (hq & 1) + 4 * i) * 2 : OOB, 0, 0);
    if (me == 0) __builtin_amdgcn_raw_buffer_store_b32(tag, rw, lane == 0 ? bp * (32 * SLAB) + EPOCH : OOB, 0, 16);
  } else {
    asm volatile("s_waitcnt vmcnt(0)" ::: "memory");
  }
  STAMP(59);
  {
    v4u ga, gb; v2u ca, cb;
    _Pragma("nounroll") for (int sp = 0; sp < SPIN_CAP; ++sp) {
      asm volatile("buffer_load_dwordx4 %0, %4, %7, 0 offen sc1\n\tbuffer_load_dwordx4 %1, %4, %7, %5 offen sc1\n\t"
                   "buffer_load_dwordx2 %2, %6, %7, 0 offen sc1\n\tbuffer_load_dwordx2 %3, %6, %7, 0 offen offset:512 sc1\n\ts_waitcnt vmcnt(0)"
                   : "=&v"(ga), "=&v"(gb), "=&v"(ca), "=&v"(cb) : "v"(vg), "s"(8192), "v"(vgc), "s"(rw) : "memory");
      const v2u f = half8(row8(fold(gb, fold(ga))));
      if (__all(f.x + 2 * tag == ca.x + cb.x && f.y == (ca.y ^ cb.y))) break;
      __builtin_amdgcn_s_sleep(1);
    }
    f32x4 y = {0.f, 0.f, 0.f, 0.f};
    y = MF16(BC(ga), BC(sel), y); y = MF16(BC(gb), BC(sel), y);
    __builtin_amdgcn_raw_buffer_store_b64(v2u{pk(y[0], y[1]), pk(y[2], y[3])}, ry, vy,
                                          4 * ((w >> 1) & 1) * 1152 + (128 * i + 64 * (hq & 1) + 32 * (w >> 2) + 16 * (w & 1)) * 2, 0);
  }
  STAMP(60);
}

extern "C" void kernel_launch(void* const* d_in, const int* in_sizes, int n_in, void* d_out, int out_size, void* d_ws, size_t ws_size,
                              hipStream_t stream) {
  gqa_fused<<<256, 512, 0, stream>>>(d_in[0], d_in[1], d_in[2], d_in[3], d_in[4], d_out, (char*)d_ws
  );
}
```

```cpp
#include <hip/hip_runtime.h>
#include <hip/hip_bf16.h>

typedef unsigned long long u64;
typedef unsigned u32;
typedef u32 v4u __attribute__((ext_vector_type(4)));
typedef u32 v2u __attribute__((ext_vector_type(2)));
typedef short s16x4 __attribute__((ext_vector_type(4)));
typedef short bf16x8 __attribute__((ext_vector_type(8)));
typedef float f32x16 __attribute__((ext_vector_type(16)));
typedef float f32x4 __attribute__((ext_vector_type(4)));
typedef float f32x2 __attribute__((ext_vector_type(2)));
typedef __bf16 b16x2 __attribute__((ext_vector_type(2)));
#define LAS __attribute__((address_space(3)))
#define GAS __attribute__((address_space(1)))
#define DI __device__ __forceinline__
typedef __amdgpu_buffer_rsrc_t rsrc_t;

#define SLABSZ 36864
constexpr u32 PG = 8192;
constexpr u32 SLAB = SLABSZ;
constexpr u32 IN9 = 16384, POUT = 24576, CMAIN = 28672, C9 = 30720, CP = 31744, EPOCH = 32256;
constexpr u32 SELF = SLABSZ > 32768 ? 32768 : 256 * SLAB, SELFS = SLABSZ > 32768 ? SLAB : 4096;
constexpr u32 WS_BYTES = 256 * SLAB + 256 * 4096;
constexpr u32 OOB = 0x80000000u;
constexpr int SPIN_CAP = 1 << 16;
#define CODE_BYTES 16128
#define PACE 6
#define PSLEEP 0
#define KPOS 2
#define SH0 4
#define SH1 5
#define SH2 6
#define SH3 7
#define HPRIO 2
#define PPOS 1
#define PEARLY 0
#define L8POS 17
#define STFROM 8
#define GSLEEP3 14
#define KPRO 0
#define N0 2
#define PL2 0
#define XB10 0
#define XB12 2
#define TSYNC 64
#define PFTOP 0
#define XPRIO 3
#define MASKP 1
#define PRECK 0
#define WSOFF 0
#define L8E 0
#define RSLEEP 0
#define ROTX 1
#define GSLEEP2 0
#define GSLEEP 18

#define STAMP(i)
#define SB() __builtin_amdgcn_sched_barrier(0)
#define HT(k, N) asm volatile("s_waitcnt vmcnt(%0) lgkmcnt(0)\n\ts_barrier" :: "i"(N) : "memory")
#define CT(k) do { SB(); asm volatile("s_waitcnt lgkmcnt(0)\n\ts_barrier" ::: "memory"); SB(); } while (0)
#define CB(k) do { SB(); asm volatile("s_barrier" ::: "memory"); SB(); } while (0)
#define HV(k, N) asm volatile("s_waitcnt vmcnt(%0)\n\ts_barrier" :: "i"(N) : "memory")
#define YV(k, N) do { SB(); asm volatile("s_waitcnt vmcnt(%0)\n\ts_barrier" :: "i"(N) : "memory"); SB(); } while (0)
#define LS(k)
#define WV(N) asm volatile("s_waitcnt vmcnt(%0)" :: "i"(N) : "memory")
#define PIN(v) asm volatile("" : "+v"(v))

template <bool NT> DI void dma(u32 lds, u32 voff, rsrc_t rs, u32 soff) {
  if constexpr (NT) asm volatile("s_mov_b32 m0, %0\n\ts_nop 0\n\tbuffer_load_dwordx4 %1, %2, %3 offen lds" :: "s"(lds), "v"(voff), "s"(rs), "s"(soff) : "memory");
  else              asm volatile("s_mov_b32 m0, %0\n\ts_nop 0\n\tbuffer_load_dwordx4 %1, %2, %3 offen lds" :: "s"(lds), "v"(voff), "s"(rs), "s"(soff) : "memory");
}
DI void st16(v4u v, u32 voff, rsrc_t rs, u32 soff) {
  asm volatile("buffer_store_dwordx4 %0, %1, %2, %3 offen sc1\n\ts_nop 1" :: "v"(v), "v"(voff), "s"(rs), "s"(soff) : "memory");
}
DI void st8(v2u v, u32 voff, rsrc_t rs, u32 soff) {
  asm volatile("buffer_store_dwordx2 %0, %1, %2, %3 offen sc1" :: "v"(v), "v"(voff), "s"(rs), "s"(soff) : "memory");
}
DI s16x4 tr4(u32 a) { return __builtin_amdgcn_ds_read_tr16_b64_v4i16((LAS s16x4*)(size_t)a); }
DI bf16x8 trd(u32 a0, u32 a1) { return __builtin_shufflevector(tr4(a0), tr4(a1), 0, 1, 2, 3, 4, 5, 6, 7); }
DI bf16x8 rd16(u32 a) { return *(LAS bf16x8*)(size_t)a; }
DI u32 pk(float a, float b) { f32x2 v = {a, b}; return __builtin_bit_cast(u32, __builtin_convertvector(v, b16x2)); }
template <int S> DI v4u pack8(const f32x16& c) {
  v4u r = {pk(c[8 * S], c[8 * S + 1]), pk(c[8 * S + 2], c[8 * S + 3]), pk(c[8 * S + 4], c[8 * S + 5]), pk(c[8 * S + 6], c[8 * S + 7])};
  PIN(r);
  return r;
}
DI f32x16 MF32(bf16x8 a, bf16x8 b, f32x16 c) { SB(); f32x16 d = __builtin_amdgcn_mfma_f32_32x32x16_bf16(a, b, c, 0, 0, 0); PIN(d); SB(); return d; }
DI f32x4 MS16(bf16x8 a, bf16x8 b, f32x4 c) { SB(); f32x4 d = __builtin_amdgcn_mfma_f32_16x16x32_bf16(a, b, c, 0, 0, 0); PIN(d); SB(); return d; }
#define MF16(a, b, c) __builtin_amdgcn_mfma_f32_16x16x32_bf16((a), (b), (c), 0, 0, 0)
#define BC(v) __builtin_bit_cast(bf16x8, (v))
template <int C> DI float dppf(float v) { return __builtin_bit_cast(float, __builtin_amdgcn_mov_dpp(__builtin_bit_cast(int, v), C, 0xF, 0xF, true)); }
template <int C> DI u32 dppu(u32 v) { return (u32)__builtin_amdgcn_mov_dpp((int)v, C, 0xF, 0xF, true); }
DI u32 xor3(u32 a, u32 b, u32 c) { return __builtin_amdgcn_bitop3_b32(a, b, c, 0x96); }
DI v2u fold(v4u d) { return v2u{(d.x + d.y + d.z) + d.w, xor3(d.x, d.y, d.z) ^ d.w}; }
DI v2u fold(v4u d, v2u a) { return v2u{(d.x + d.y + a.x) + d.z + d.w, xor3(xor3(d.x, d.y, a.y), d.z, d.w)}; }
DI v2u row8(v2u v) { return v2u{v.x + dppu<0x128>(v.x), v.y ^ dppu<0x128>(v.y)}; }
DI v2u half8(v2u v) {
  v.x += dppu<0x141>(v.x); v.y ^= dppu<0x141>(v.y); v.x += dppu<0x4E>(v.x); v.y ^= dppu<0x4E>(v.y);
  v.x += dppu<0xB1>(v.x); v.y ^= dppu<0xB1>(v.y);
  return v;
}

__global__ void __launch_bounds__(512, 2)
gqa_fused(const void* x_, const void* kc_, const void* vc_, const void* wq_, const void* wo_, void* out_, char* ws_, char* pad_
) {
  __shared__ __attribute__((aligned(1024))) char smem[20 * PG];
  STAMP(0);
  typedef u64 v8q __attribute__((ext_vector_type(8)));
  u64 pc0; v8q ka;
  asm volatile("s_getpc_b64 %0\n\ts_load_dwordx16 %1, %2, 0" : "=&s"(pc0), "=&s"(ka) : "s"(__builtin_amdgcn_kernarg_segment_ptr()));
  SB();
  const u32 L = (u32)(size_t)(LAS char*)smem;
  const u32 tid = threadIdx.x, lane = tid & 63;
  const u32 w = __builtin_amdgcn_readfirstlane(tid >> 6), g = w & 3;
  const u32 b = blockIdx.x, hq = b & 7, bp = b >> 5, i = (b >> 3) & 3, h = 4 * hq + i;
  const u32 me = hq * 4 + i, row = lane >> 4, col = lane & 15;

  const u32 xr = 8 * g + (lane >> 3);
  u32 vx = xr * 1152 + 16 * ((lane & 7) ^ ((xr >> 1) & 7));
  const u32 pr = 4 * g + row;
  const u32 pc = col ^ ((row << 2) | g);
  u32 vq = pr * 8192 + 16 * pc;
  const u32 Lg = L + 1024 * g;
  u32 ks = 3 * ((bp + i) % 3) + ROTX * hq; ks = ks >= 9 ? ks - 9 : ks;
  const u32 sx = bp * (64 * 1152), sq = 256 * h;
  u32 n2 = w < 4 ? N0 : 0, ns = 0;
  u32 ox = sx + ks * 128, oq = sq + ks * (64 * 8192), lx = Lg + 8 * PG, lq = Lg;
  u32 vx2 = vx + 32 * 1152, vq1 = vq + 16 * 8192, vq2 = vq + 32 * 8192, vq3 = vq + 48 * 8192;
  PIN(vx); PIN(vq); PIN(vx2); PIN(vq1); PIN(vq2); PIN(vq3);
  asm volatile("s_waitcnt lgkmcnt(0)" : "+s"(ka), "+s"(ox), "+s"(oq), "+s"(lx), "+s"(lq), "+s"(n2));
  const u64 x = ka[0], kc = ka[1], vc = ka[2], wq = ka[3], wo = ka[4], out = ka[5], ws = ka[6];
  const rsrc_t rx = __builtin_amdgcn_make_buffer_rsrc((void*)x, 0, 512 * 1152, 0x00020000);
  const rsrc_t rq = __builtin_amdgcn_make_buffer_rsrc((void*)wq, 0, 576 * 8192, 0x00020000);
  auto step = [&]() {
    asm volatile("s_mov_b32 m0, %0\n\ts_nop 0\n\tbuffer_load_dwordx4 %4, %10, %2 offen lds\n\t"
                 "s_add_u32 m0, %0, 0x1000\n\ts_nop 0\n\tbuffer_load_dwordx4 %5, %10, %2 offen lds\n\t"
                 "s_mov_b32 m0, %1\n\ts_nop 0\n\tbuffer_load_dwordx4 %6, %11, %3 offen lds\n\t"
                 "s_add_u32 m0, %1, 0x1000\n\ts_nop 0\n\tbuffer_load_dwordx4 %7, %11, %3 offen lds\n\t"
                 "s_add_u32 m0, %1, 0x2000\n\ts_nop 0\n\tbuffer_load_dwordx4 %8, %11, %3 offen lds\n\t"
                 "s_add_u32 m0, %1, 0x3000\n\ts_nop 0\n\tbuffer_load_dwordx4 %9, %11, %3 offen lds"
                 :: "s"(lx), "s"(lq), "s"(ox), "s"(oq), "v"(vx), "v"(vx2), "v"(vq), "v"(vq1), "v"(vq2), "v"(vq3), "s"(rx), "s"(rq) : "scc", "memory");
    ks = ks == 8 ? 0 : ks + 1; ++ns;
    ox = sx + ks * 128; oq = sq + ks * (64 * 8192); lx = Lg + (8 + (ns & 3)) * PG; lq = Lg + (ns & 3) * (2 * PG);
    asm volatile("" : "+s"(ox), "+s"(oq), "+s"(lx), "+s"(lq));
  };
  _Pragma("nounroll") while (ns < n2) step();
  const rsrc_t rw = __builtin_amdgcn_make_buffer_rsrc((void*)ws, 0, WS_BYTES, 0x00020000);

  const rsrc_t ro = __builtin_amdgcn_make_buffer_rsrc((void*)wo, 0, 4096 * 1152, 0x00020000);
  const u32 hh = lane >> 5, l31 = lane & 31, q4 = col >> 2, p4 = lane & 3;
  u32 tag, vo, v16;
  auto wo_off = [&](u32 bb_, int n) {
    const u32 un = (n >> 1) ^ (bb_ >> 5);
    return ((bb_ & 7) * 512 + (un >> 2) * 256 + (n & 1) * 128 + (un & 3) * 32) * 1152 + 256 * ((bb_ >> 3) & 3);
  };

  if (w >= 4) {
    const u32 mt = g >> 1;
    u32 aq[8][2], ax[2], ak[8];
    const u32 xrow = 16 * g + col, key = 16 * (col >> 3) + (col & 7);
    ax[0] = L + 8 * PG + 128 * xrow + 16 * (row ^ ((xrow >> 1) & 7)); ax[1] = ax[0] ^ 64;
    PIN(ax[0]); PIN(ax[1]);
#pragma unroll
    for (int t = 0; t < 2; ++t) {
      aq[0][t] = L + 256 * (8 * row + 4 * t + q4) + 16 * ((p4 >> 1) ^ ((q4 << 2) | ((2 * row + t) & 3))) + 8 * (p4 & 1);
      PIN(aq[0][t]);
#pragma unroll
      for (int n = 1; n < 8; ++n) { aq[n][t] = aq[0][t] ^ (32 * n); PIN(aq[n][t]); }
    }
    u32 vcs, vo1, sw[6], Lw = Lg;
    const u32 sp0 = b * SLAB + POUT + g * 1024, sp1 = b * SLAB + CP + g * 128, sp2 = SELF + b * SELFS + g * 1024;
    auto late_setup = [&]() {
      ak[0] = L + (12 + mt) * PG + 256 * key + 16 * ((row >> 1) ^ (((key & 3) << 2) | ((key >> 2) & 3))) + 8 * (row & 1);
      PIN(ak[0]);
#pragma unroll
      for (int n = 1; n < 8; ++n) { ak[n] = ak[0] ^ (32 * n); PIN(ak[n]); }
      vcs = col < 4 ? (row & 1) * 64 + (row >> 1) * 32 + col * 8 : OOB; v16 = lane * 16; vo = pr * 1152 + 16 * pc; vo1 = vo + 16 * 1152;
      PIN(vcs); PIN(v16); PIN(vo); PIN(vo1);
      u32 bs = b;
      asm volatile("" : "+s"(bs), "+s"(Lw) : "s"(sp0), "s"(sp1), "s"(sp2));
#pragma unroll
      for (int n = 0; n < 6; ++n) { sw[n] = wo_off(bs, n + 10); asm volatile("" : "+s"(sw[n])); }
    };
    auto ldw = [&](int n, int pg) {
      asm volatile("s_add_u32 m0, %0, %1\n\ts_nop 0\n\tbuffer_load_dwordx4 %3, %5, %6 offen lds\n\t"
                   "s_add_u32 m0, %0, %2\n\ts_nop 0\n\tbuffer_load_dwordx4 %4, %5, %6 offen lds"
                   :: "s"(Lw), "i"(pg * PG), "i"(pg * PG + 4096), "v"(vo), "v"(vo1), "s"(ro), "s"(sw[n - 10]) : "scc", "memory");
    };
    const f32x4 z4 = {0.f, 0.f, 0.f, 0.f};
    f32x4 qa[8];
#pragma unroll
    for (int n = 0; n < 8; ++n) qa[n] = z4;
    bf16x8 A[16], B[2], kf[2][4];
    auto rA = [&](int sl, int n) { const u32 o = (2 * sl + (n >> 3)) * PG; A[n] = trd(aq[n & 7][0] + o, aq[n & 7][1] + o); };
    auto rB = [&](int sl, int k) { B[k] = rd16(ax[k] + sl * PG); };
    auto rK = [&](int T, int ks) {
      const s16x4 lo = *(LAS s16x4*)(size_t)(ak[2 * ks + T] + T * 2048), hi = *(LAS s16x4*)(size_t)(ak[2 * ks + 1 - T] + T * 2048);
      kf[T][ks] = __builtin_shufflevector(lo, hi, 0, 1, 2, 3, 4, 5, 6, 7);
    };
    bf16x8 Bc;
    auto head = [&](int sl, bool carry) {
      rB(sl, 0);
#pragma unroll
      for (int n = 0; n < 6; ++n) { if (carry) qa[n + 2] = MS16(A[n + 10], Bc, qa[n + 2]); else SB(); rA(sl, n); }
      rB(sl, 1);
    };
    auto p1 = [&](int t) {
      head(t & 3, t > 0);
#pragma unroll
      for (int n = 0; n < 10; ++n) { qa[n & 7] = MS16(A[n], B[n >> 3], qa[n & 7]); rA(t & 3, n + 6); }
      Bc = B[1];
    };
    late_setup(); CB(0); p1(0); CB(1); p1(1); CB(2); p1(2);
    asm volatile("s_buffer_load_dword %0, %1, %2\n\ts_waitcnt lgkmcnt(0)" : "=s"(tag) : "s"(rw), "s"(bp * (32 * SLAB) + EPOCH) : "memory");
    tag += 1;
    CB(3); p1(3); CB(4); p1(4); CB(5); p1(5); CB(6); p1(6); CB(7); p1(7);
    CB(8);
    head(0, true);
#pragma unroll
    for (int n = 0; n < 8; ++n) { qa[n] = MS16(A[n], B[0], qa[n]); if (n < 2) rA(0, n + 6); else if (n < 6) rK(0, n - 2); }
    CB(9);
    f32x4 sa = z4, sb = z4;
    {
      v4u q[4];
      auto pq = [&](int ks) { q[ks] = v4u{pk(qa[2 * ks][0], qa[2 * ks][1]), pk(qa[2 * ks][2], qa[2 * ks][3]), pk(qa[2 * ks + 1][0], qa[2 * ks + 1][1]), pk(qa[2 * ks + 1][2], qa[2 * ks + 1][3])}; PIN(q[ks]); };
#pragma unroll
      for (int n = 8; n < 14; ++n) { rA(0, n); SB(); }
      qa[0] = MS16(A[8], B[1], qa[0]); rA(0, 14);
      qa[1] = MS16(A[9], B[1], qa[1]); rA(0, 15);
      qa[2] = MS16(A[10], B[1], qa[2]); rK(1, 0);
      qa[3] = MS16(A[11], B[1], qa[3]); rK(1, 1);
      qa[4] = MS16(A[12], B[1], qa[4]); rK(1, 2);
      qa[5] = MS16(A[13], B[1], qa[5]); rK(1, 3);
      pq(0);
      qa[6] = MS16(A[14], B[1], qa[6]);
      qa[7] = MS16(A[15], B[1], qa[7]);
      pq(1);
      sa = MS16(kf[0][0], BC(q[0]), sa); sb = MS16(kf[1][0], BC(q[0]), sb);
      pq(2);
      sa = MS16(kf[0][1], BC(q[1]), sa); sb = MS16(kf[1][1], BC(q[1]), sb);
      pq(3);
      sa = MS16(kf[0][2], BC(q[2]), sa); sb = MS16(kf[1][2], BC(q[2]), sb);
      sa = MS16(kf[0][3], BC(q[3]), sa); sb = MS16(kf[1][3], BC(q[3]), sb);
    }
    __builtin_amdgcn_s_setprio(XPRIO);
    if (XB10 == 0) CB(10);
    {
      float mx, m2;
      asm volatile("s_nop 7\n\tv_max3_f32 %0, %2, %3, %4\n\tv_max3_f32 %1, %5, %6, %7\n\tv_max3_f32 %0, %0, %8, %9\n\tv_max_f32 %0, %0, %1\n\t"
                   "v_mov_b32 %1, %0\n\ts_nop 1\n\tv_permlane16_swap_b32 %0, %1\n\tv_max_f32 %0, %0, %1\n\t"
                   "v_mov_b32 %1, %0\n\ts_nop 1\n\tv_permlane32_swap_b32 %0, %1\n\tv_max_f32 %0, %0, %1"
                   : "=&v"(mx), "=&v"(m2) : "v"(sa[0]), "v"(sa[1]), "v"(sa[2]), "v"(sa[3]), "v"(sb[0]), "v"(sb[1]), "v"(sb[2]), "v"(sb[3]));
      if (XB10 == 1) CB(10);
      const float c = 0.08838834764831845f * 1.4426950408889634f, mc = -mx * c;
      float e[8], z2;
#pragma unroll
      for (int n = 0; n < 4; ++n) { e[n] = __builtin_amdgcn_exp2f(__builtin_fmaf(sa[n], c, mc)); e[4 + n] = __builtin_amdgcn_exp2f(__builtin_fmaf(sb[n], c, mc)); }
      float z = ((e[0] + e[2]) + (e[4] + e[6])) + ((e[1] + e[3]) + (e[5] + e[7]));
      PIN(z);
      if (XB10 == 2) CB(10);
      if (XB12 == 0) CB(12);
      asm volatile("v_mov_b32 %1, %0\n\ts_nop 1\n\tv_permlane16_swap_b32 %0, %1\n\tv_add_f32 %0, %0, %1\n\t"
                   "v_mov_b32 %1, %0\n\ts_nop 1\n\tv_permlane32_swap_b32 %0, %1\n\tv_add_f32 %0, %0, %1" : "+v"(z), "=&v"(z2));
      const float r = __builtin_amdgcn_rcpf(z);
      const v4u pv = {pk(e[0] * r, e[1] * r), pk(e[2] * r, e[3] * r), pk(e[4] * r, e[5] * r), pk(e[6] * r, e[7] * r)};
      if (XB12 == 3) { v4u pp = pv; PIN(pp); CB(12); }
      st16(pv, v16, rw, sp0);
      if (XB12 == 1) CB(12);
      v2u ck = half8(row8(fold(pv)));
      ck.x += tag; st8(ck, vcs, rw, sp1);
      *(LAS v4u*)(size_t)(L + 12 * PG + g * 1024 + v16) = pv;
    }
    STAMP(61);
    if (XB12 == 2) CB(12);
    u32 vp = (row & 1) * 1024 + (row >> 1) * 256 + col * 16, vpc = (row & 1) * 128 + (row >> 1) * 64 + 24, pl = L + 2 * PG + g * 4096 + lane * 16;
    PIN(vp); PIN(vpc); PIN(pl);
    ldw(10, 1); ldw(11, 8);
    {
      v4u c0, c1;
      const u32 spl = ((bp << 5) | ((g ^ ((bp >> 2) << 1)) << 3) | hq) * SLAB, lp = L + 2 * PG + g * 4096;
      auto ldp = [&]() {
        asm volatile("s_mov_b32 m0, %2\n\ts_nop 0\n\tbuffer_load_dwordx4 %4, %6, %7 offen sc1 lds\n\t"
                     "buffer_load_dwordx4 %4, %6, %7 offen offset:2048 sc1 lds\n\t"
                     "s_mov_b32 m0, %3\n\ts_nop 0\n\tbuffer_load_dwordx4 %4, %6, %7 offen offset:512 sc1 lds\n\t"
                     "buffer_load_dwordx4 %4, %6, %7 offen offset:2560 sc1 lds\n\t"
                     "buffer_load_dwordx4 %0, %5, %6, %8 offen sc1\n\tbuffer_load_dwordx4 %1, %5, %6, %8 offen offset:256 sc1"
                     : "=&v"(c0), "=&v"(c1)
                     : "s"(lp), "s"(lp + 512), "v"(vp), "v"(vpc), "s"(rw), "s"(spl + POUT), "s"(spl + CP) : "memory");
      };
      HV(14, 63); ldw(12, 14); ldw(13, 15);
      HT(15, 63); ldw(14, 9); ldw(15, 10);
      if (PPOS == 0) ldp();
      HV(16, PPOS == 0 ? 14 : 8);
      if (PPOS == 1) { __builtin_amdgcn_s_sleep(PSLEEP); ldp(); }
      HV(17, PPOS <= 1 ? 10 : 4);
      if (PPOS == 2) { __builtin_amdgcn_s_sleep(PSLEEP); ldp(); }
      HV(18, 6);
      asm volatile("s_waitcnt vmcnt(0)" : "+v"(c0), "+v"(c1) :: "memory");
      STAMP(62);
      _Pragma("nounroll") for (int sp = 0; sp < SPIN_CAP; ++sp) {
        const v4u p0 = *(LAS v4u*)(size_t)pl, p1 = *(LAS v4u*)(size_t)(pl + 1024), p2 = *(LAS v4u*)(size_t)(pl + 2048), p3 = *(LAS v4u*)(size_t)(pl + 3072);
        const v2u f0 = fold(p1, fold(p0)), f1 = fold(p3, fold(p2));
        const v2u f = half8(row8(v2u{f0.x + f1.x, f0.y ^ f1.y}));
        if (__all(f.x + 4 * tag == (c0.x + c0.z) + (c1.x + c1.z) && f.y == (xor3(c0.y, c0.w, c1.y) ^ c1.w))) break;
        ldp();
        asm volatile("s_waitcnt vmcnt(0)" : "+v"(c0), "+v"(c1) :: "memory");
      }
    }
    __builtin_amdgcn_s_setprio(0);
    asm volatile("s_barrier" ::: "memory");
    STAMP(57);
    const rsrc_t ry = __builtin_amdgcn_make_buffer_rsrc((void*)out, 0, 512 * 1152, 0x00020000);
    const u32 one = (col & 1) ? 0x3F800000u : 0x00003F80u;
    v4u sel = {(col >> 1) == 0 ? one : 0u, (col >> 1) == 1 ? one : 0u, (col >> 1) == 2 ? one : 0u, (col >> 1) == 3 ? one : 0u};
    const u32 yrow = 64 * bp + 32 * (hq >> 2) + 16 * ((hq >> 1) & 1) + (col & 3) + 8 * ((col >> 2) & 1) + 4 * (g >> 1);
    u32 vy = col < 8 ? yrow * 1152 + 8 * row + (128 * i + 64 * (hq & 1) + 16 * (g & 1)) * 2 : OOB;
    u32 vg = row == hq ? OOB : row * 2048 + g * 256 + col * 16, vgc = row == hq ? OOB : row * 256 + g * 32;
    u32 vh = row + 4 == hq ? OOB : row * 2048 + g * 256 + col * 16 + 8192, vhc = row + 4 == hq ? OOB : row * 256 + g * 32 + 1024;
    u32 tags = row == (hq & 3) ? 2 * tag : 4 * tag, aown = L + 13 * PG + g * 256 + col * 16;
    v4u sel0 = row == 0 ? sel : v4u{0u, 0u, 0u, 0u};
    PIN(sel); PIN(vy); PIN(vg); PIN(vgc); PIN(vh); PIN(vhc); PIN(tags); PIN(aown); PIN(sel0);
    asm volatile("s_barrier" ::: "memory");
    __builtin_amdgcn_s_sleep(GSLEEP);
    STAMP(59);
    {
      const u32 sg = b * SLAB;
      v4u g0, g1, g2, g3; v2u c0, c1, c2, c3;
      const f32x4 z4 = {0.f, 0.f, 0.f, 0.f};
      f32x4 ya, yb;
#define LOAD_G "buffer_load_dwordx4 %0, %8, %12, %13 offen sc1\n\tbuffer_load_dwordx4 %1, %10, %12, %13 offen sc1\n\t" \
               "buffer_load_dwordx4 %2, %8, %12, %13 offen offset:1024 sc1\n\tbuffer_load_dwordx4 %3, %10, %12, %13 offen offset:1024 sc1\n\t" \
               "buffer_load_dwordx2 %4, %9, %12, %14 offen sc1\n\tbuffer_load_dwordx2 %5, %11, %12, %14 offen sc1\n\t" \
               "buffer_load_dwordx2 %6, %9, %12, %14 offen offset:128 sc1\n\tbuffer_load_dwordx2 %7, %11, %12, %14 offen offset:128 sc1\n\ts_waitcnt vmcnt(0)"
      f32x4 oa = MF16(rd16(aown), BC(sel0), z4), ob = MF16(rd16(aown + 1024), BC(sel0), z4);
      PIN(oa); PIN(ob);
      asm volatile(LOAD_G : "=&v"(g0), "=&v"(g1), "=&v"(g2), "=&v"(g3), "=&v"(c0), "=&v"(c1), "=&v"(c2), "=&v"(c3)
                   : "v"(vg), "v"(vgc), "v"(vh), "v"(vhc), "s"(rw), "s"(sg), "s"(sg + CMAIN) : "memory");
      STAMP(55);
      _Pragma("nounroll") for (int sp = 0; sp < SPIN_CAP; ++sp) {
        ya = MF16(BC(g0), BC(sel), oa); yb = MF16(BC(g2), BC(sel), ob);
        ya = MF16(BC(g1), BC(sel), ya); yb = MF16(BC(g3), BC(sel), yb);
        PIN(ya); PIN(yb);
        const v2u f0 = fold(g1, fold(g0)), f1 = fold(g3, fold(g2));
        const v2u f = half8(row8(v2u{f0.x + f1.x, f0.y ^ f1.y}));
        const u64 bad = __builtin_amdgcn_ballot_w64(((f.x + tags) ^ ((c0.x + c1.x) + (c2.x + c3.x))) | (f.y ^ xor3(c0.y, c1.y, c2.y) ^ c3.y));
        if (bad == 0) break;
        __builtin_amdgcn_s_sleep(RSLEEP);
        asm volatile("s_mov_b64 exec, %15\n\t" LOAD_G "\n\ts_mov_b64 exec, -1"
                     : "+v"(g0), "+v"(g1), "+v"(g2), "+v"(g3), "+v"(c0), "+v"(c1), "+v"(c2), "+v"(c3)
                     : "v"(vg), "v"(vgc), "v"(vh), "v"(vhc), "s"(rw), "s"(sg), "s"(sg + CMAIN), "s"(bad) : "memory");
      }
      __builtin_amdgcn_raw_buffer_store_b64(v2u{pk(ya[0], ya[1]), pk(ya[2], ya[3])}, ry, vy, 0, 0);
      __builtin_amdgcn_raw_buffer_store_b64(v2u{pk(yb[0], yb[1]), pk(yb[2], yb[3])}, ry, vy, 64, 0);
    }
  } else {
    const rsrc_t rk = __builtin_amdgcn_make_buffer_rsrc((void*)kc, 0, 256 * 8192, 0x00020000);
    const rsrc_t rv = __builtin_amdgcn_make_buffer_rsrc((void*)vc, 0, 256 * 8192, 0x00020000);
    u32 vkv = pr * 256 + 16 * pc, vo9 = (pr + 64 * (pc >> 3)) * 1152 + 16 * (pc & 7);
    vo = pr * 1152 + 16 * pc;
    PIN(vkv); PIN(vo); PIN(vo9);
    u32 bs = b;
    asm volatile("" : "+s"(bs));
    auto ldk = [&](u32 n) {
      const u32 o = ((2 * bp + n) * 16 + (h >> 1)) * 8192;
      dma<false>(Lg + (12 + n) * PG, vkv, rk, o); dma<false>(Lg + (12 + n) * PG + 4096, vkv, rk, o + 4096);
    };
    auto ldv = [&](u32 n) {
      const u32 o = ((2 * bp + (n & 1)) * 16 + 2 * hq + ((n >> 1) ^ (bp >> 2))) * 8192;
      dma<false>(Lg + (16 + n) * PG, vkv, rv, o); dma<false>(Lg + (16 + n) * PG + 4096, vkv, rv, o + 4096);
    };
    auto ldw = [&](int n, int pg) { const u32 o = wo_off(bs, n); dma<false>(Lg + pg * PG, vo, ro, o); dma<false>(Lg + pg * PG + 4096, vo, ro, o + 16 * 1152); };
    auto ldn = [&](int n, int pg) {
      const u32 o = (h * 128 + 32 * n) * 1152 + 1024;
      dma<false>(Lg + pg * PG, vo9, ro, o); dma<false>(Lg + pg * PG + 4096, vo9, ro, o + 16 * 1152);
    };
    const u32 mys = (bp & 3) == 0 ? SH0 : (bp & 3) == 1 ? SH1 : (bp & 3) == 2 ? SH2 : SH3;
    u32 aw[2][2], av[8], a9[8], an[4];
    const u32 mt = g >> 1;
    u32 vcm = (col & 7) < 4 ? (col >> 3) * (2 * SLAB) + row * 32 + (col & 3) * 8 : OOB, ap = L + 2 * PG + lane * 16;
    u32 ao = L + 12 * PG + (2 * mt + (row & 1)) * 1024 + hh * 256 + col * 16;
    u32 v9 = (row << 3) * SLAB + col * 16, vc9 = (col & 7) < 4 ? (row << 3) * SLAB + (col >> 3) * (2 * SLAB) + (col & 3) * 8 : OOB;
    v16 = lane * 16;
    u32 aown = L + 13 * PG + (g & 1) * 1024 + lane * 16;
    const u32 ownq = (g >> 1) == (hq & 1) ? hq >> 1 : 4;
    PIN(vcm); PIN(ap); PIN(ao); PIN(v9); PIN(vc9); PIN(v16); PIN(aown);
#pragma unroll
    for (int t = 0; t < 2; ++t) {
      aw[0][t] = L + 256 * (8 * hh + 4 * t + q4) + 16 * ((4 * g + 2 * (row & 1) + (p4 >> 1)) ^ ((q4 << 2) | (2 * hh + t))) + 8 * (p4 & 1);
      PIN(aw[0][t]);
      aw[1][t] = aw[0][t] + 8 * PG; an[t] = (aw[0][t] ^ (128 * mt)) + 2 * PG; an[t + 2] = an[t] ^ 128;
      PIN(aw[1][t]); PIN(an[t]); PIN(an[t + 2]);
    }
    {
      u32 arow = L + 16 * PG + 256 * l31 + 16 * (hh ^ (((l31 & 3) << 2) | ((l31 >> 2) & 3)));
      u32 a9b = arow + (mt + 2 * ((i >> 1) ^ (bp >> 2))) * PG;
      arow ^= 64 * (bp & 3);
      PIN(arow); PIN(a9b);
#pragma unroll
      for (int n = 0; n < 8; ++n) { av[n] = arow ^ (32 * n); a9[n] = a9b ^ (32 * n); PIN(av[n]); PIN(a9[n]); }
    }
    const u32 so = ((bp << 5) | (i << 3) | (g >> 1)) * SLAB, s9 = ((bp << 5) | (mt * 4 + (g & 1))) * SLAB;
    const u32 sd0 = so + hq * 2048 + (g & 1) * 1024, sd1 = sd0 + 2 * SLAB, sd2 = sd0 + 4 * SLAB, sd3 = sd0 + 6 * SLAB;
    const u32 sc0 = so + CMAIN + hq * 256 + (g & 1) * 128, sc1 = sc0 + 4 * SLAB;
    const u32 s9a = s9 + IN9 + me * 256, s9b = s9a + 2 * SLAB, s9c = s9 + C9 + me * 32;
    asm volatile("" :: "s"(sd0), "s"(sd1), "s"(sd2), "s"(sd3), "s"(sc0), "s"(sc1), "s"(s9a), "s"(s9b), "s"(s9c));
    u32 sy[4], Ly = Lg, vo1 = vo + 16 * 1152;
#pragma unroll
    for (int n = 0; n < 4; ++n) { sy[n] = wo_off(bs, n + 6); asm volatile("" : "+s"(sy[n])); }
    asm volatile("" : "+s"(Ly)); PIN(vo1);
    auto ldh = [&](int n, int pg, int k) {
      asm volatile("s_add_u32 m0, %0, %1\n\ts_nop 0\n\tbuffer_load_dwordx4 %2, %3, %4 offen lds"
                   :: "s"(Ly), "i"(pg * PG + k * 4096), "v"(k ? vo1 : vo), "s"(ro), "s"(sy[n - 6]) : "scc", "memory");
    };
    SB();
    dma<false>(Lg + (16 + (i ^ 1)) * PG, vo9, ro, (h * 128 + 16 * (bp & 3)) * 1152 + 1024);
    if (KPRO) ldk(i & 1);
    step();
    {
      const rsrc_t rc = __builtin_amdgcn_make_buffer_rsrc((void*)(pc0 & ~255ull), 0, CODE_BYTES, 0x00020000);
      const rsrc_t ry = __builtin_amdgcn_make_buffer_rsrc((void*)out, 0, 512 * 1152, 0x00020000);
      asm volatile("s_buffer_load_dword s100, %0, %1\n\ts_buffer_load_dword s101, %2, %3\n\ts_buffer_load_dword s101, %2, %3 offset:64\n\t"
                   "s_nop 0"
                   :: "s"(rw), "s"(bp * (32 * SLAB) + EPOCH), "s"(rc), "s"(((b >> 3) * 4 + g) * 128), "s"(ry), "s"(bp * (64 * 1152)) : "s100", "s101", "memory");
    }
    HV(0, KPRO ? 15 : 13);
    step();
    HV(1, KPRO ? 15 : 13);
    u32 nb = 2;
    _Pragma("nounroll") do {
      if (nb == ns - 2) { asm volatile("s_waitcnt vmcnt(6)\n\ts_barrier" ::: "memory"); LS(nb); ++nb; }
      if (ns == 8) ldk((i & 1) ^ 1);
      step();
      if (ns == mys + 1) {
        if (!KPRO) ldk(i & 1);
        ldv(i); ldw(0, 14); ldw(1, 15);
        asm volatile("s_waitcnt vmcnt(%0)\n\ts_barrier" :: "i"(KPRO ? 12 : 14) : "memory"); LS(nb);
        asm volatile("s_waitcnt vmcnt(%0)\n\ts_barrier" :: "i"(KPRO ? 6 : 8) : "memory"); LS(nb + 1);
        nb += 2;
      }
    } while (ns < 9);
    if (nb == 7) { asm volatile("s_waitcnt vmcnt(8)\n\ts_barrier" ::: "memory"); LS(7); }
    ldv(i ^ 1); ldv(i ^ 2); ldv(i ^ 3);
    asm volatile("s_waitcnt lgkmcnt(0)\n\ts_mov_b32 %0, s100" : "=s"(tag) :: "memory");
    tag += 1;
    HV(8, 8); ldw(2, 9); ldw(3, 10);
    HV(9, 10); ldn(0, 2); ldn(1, 3); ldw(4, 4); ldw(5, 5);

    f32x16 zero;
#pragma unroll
    for (int n = 0; n < 16; ++n) zero[n] = 0.f;
    bf16x8 vf[2][2], bw[2][2];
    f32x16 u[2][2];
    v4u uu[4][2][2];
    auto rV = [&](int j, int k, int bb) { vf[k][bb] = rd16(av[2 * (j & 3) + k] + (2 * (j >> 2) + bb) * PG); };
    auto rW = [&](int pg, int k, int hb) { const u32 o = (pg & 7) * PG + k * 4096; bw[k][hb] = trd(aw[pg >> 3][0] + o, aw[pg >> 3][1] + o); };
    auto pku = [&](int hs, int bb, int k, const f32x16& a) { uu[hs][bb][k] = k ? pack8<1>(a) : pack8<0>(a); };
    auto second = [&](int n) { u[n >> 1][n & 1] = MF32(vf[1][n & 1], bw[1][n >> 1], u[n >> 1][n & 1]); };
    auto unit = [&](int j, int pa, int pb, auto carry, auto f) {
      const bool fresh = (j & 3) == 0;
      rV(j, 0, 0); rW(pa, 0, 0);
      carry(0); rV(j, 0, 1); rW(pb, 0, 1); f(0);
      carry(1); rW(pa, 1, 0); f(1);
      carry(2); rV(j, 1, 0); f(2);
      carry(3); rV(j, 1, 1); rW(pb, 1, 1); f(3);
      const f32x16 o00 = u[0][0], o01 = u[0][1], o10 = u[1][0], o11 = u[1][1];
      u[0][0] = MF32(vf[0][0], bw[0][0], fresh ? zero : u[0][0]); f(4);
      if (j == 4) { pku(0, 0, 0, o00); pku(0, 0, 1, o00); }
      u[0][1] = MF32(vf[0][1], bw[0][0], fresh ? zero : u[0][1]); f(5);
      if (j == 4) { pku(0, 1, 0, o01); pku(0, 1, 1, o01); }
      u[1][0] = MF32(vf[0][0], bw[0][1], fresh ? zero : u[1][0]); f(6);
      if (j == 4) { pku(1, 0, 0, o10); pku(1, 0, 1, o10); }
      u[1][1] = MF32(vf[0][1], bw[0][1], fresh ? zero : u[1][1]); f(7);
      if (j == 4) { pku(1, 1, 0, o11); pku(1, 1, 1, o11); }
    };
    auto nop = [](int) {};
    YV(10, 8);
    unit(0, 14, 15, [](int) { SB(); }, [&](int k) { if (k >= 4) ldh(6 + ((k - 4) >> 1), 6 + ((k - 4) >> 1), k & 1); });
    unit(1, 9, 10, second, [&](int k) { if (k >= 4) ldh(8 + ((k - 4) >> 1), k < 6 ? 11 : 0, k & 1); });
    bf16x8 va[8], wb[8]; f32x16 u9; v4u u9a, u9b;
    auto r9 = [&](int s) { va[s] = rd16(a9[s]); wb[s] = trd(an[(s >> 2) * 2] + (s & 3) * 4096, an[(s >> 2) * 2 + 1] + (s & 3) * 4096); };
    YV(12, 8);
#pragma unroll
    for (int s = 0; s < 4; ++s) { r9(s); second(s); }
#pragma unroll
    for (int s = 0; s < 4; ++s) { r9(s + 4); u9 = MF32(va[s], wb[s], s ? u9 : zero); }
    unit(2, 4, 5, [&](int s) { u9 = MF32(va[s + 4], wb[s + 4], u9); }, [&](int k) { if (k == 6) u9a = pack8<0>(u9); if (k == 7) u9b = pack8<1>(u9); });
    YV(14, 4); unit(3, 6, 7, second, nop);
    bf16x8 pf[8], po0, po1;
    auto rP = [&](int n, int hs, int bb, int k) { pf[n] = rd16(ap + hs * 4096 + bb * 2048 + k * 1024); };
    f32x16 o9; v4u e0, e1; v2u f0, f1;
    auto rd9 = [&](int k) { if (k == 2) { po0 = rd16(ao); po1 = rd16(ao + 512); } };
    auto mm9 = [&]() { o9 = MF32(po0, BC(u9a), zero); o9 = MF32(po1, BC(u9b), o9); };
    auto st9 = [&](int k) {
      if (k == 1) e0 = pack8<0>(o9); else if (k == 2) e1 = pack8<1>(o9); else if (k == 4) st16(e0, v9, rw, s9a);
      else if (k == 5) { f0 = fold(e0); PIN(f0); } else if (k == 6) st16(e1, v9, rw, s9b); else if (k == 7) { f1 = fold(e1); PIN(f1); } };
    auto ck9 = [&](int k) {
      if (k == 4) { f0 = row8(f0); f1 = row8(f1); f0 = col < 8 ? f0 : f1; PIN(f0); }
      else if (k == 5) { f0 = half8(f0); f0.x += tag; PIN(f0); } else if (k == 6) st8(f0, vc9, rw, s9c); };
    YV(15, 0); unit(4, 11, 0, second, nop);
    CB(16); unit(5, 1, 8, second, rd9); mm9();
    CB(17); unit(6, 14, 15, second, st9);
    CB(18); unit(7, 9, 10, second, ck9);
    STAMP(63);
    SB(); asm volatile("s_barrier" ::: "memory"); SB();
    rP(0, 0, 0, 0); rP(1, 0, 0, 1);
    second(0); rP(2, 1, 0, 0); rP(3, 1, 0, 1);
    second(2); rP(4, 2, 0, 0); rP(5, 2, 0, 1);
    second(1); rP(6, 3, 0, 0); rP(7, 3, 0, 1);
    second(3); pku(2, 0, 0, u[0][0]);
    f32x16 o0, o1;
#pragma unroll
    for (int n = 0; n < 8; ++n) {
      o0 = MF32(pf[n], BC(uu[n >> 1][0][n & 1]), n ? o0 : zero); rP(n, n >> 1, 1, n & 1);
      if (n == 0) pku(2, 0, 1, u[0][0]);
      if (n == 1) pku(3, 0, 0, u[1][0]);
      if (n == 2) pku(3, 0, 1, u[1][0]);
      if (n >= 3 && n < 7) pku(2 + ((n - 3) >> 1), 1, (n - 3) & 1, u[(n - 3) >> 1][1]);
    }
    auto pub = [&](int q, v4u d, u32 so_) { if (ownq == q) *(LAS v4u*)(size_t)aown = d; else st16(d, v16, rw, so_); };
    v4u d0, d1;
#pragma unroll
    for (int n = 0; n < 8; ++n) {
      o1 = MF32(pf[n], BC(uu[n >> 1][1][n & 1]), n ? o1 : zero);
      if (n == 1) { d0 = pack8<0>(o0); pub(0, d0, sd0); } else if (n == 2) { d1 = pack8<1>(o0); pub(1, d1, sd1); }
      else if (n == 3) { f0 = fold(d0); PIN(f0); } else if (n == 4) { f1 = fold(d1); PIN(f1); }
      else if (n == 5) { f0 = row8(f0); f1 = row8(f1); f0 = col < 8 ? f0 : f1; PIN(f0); }
      else if (n == 7) { f0 = half8(f0); f0.x += tag; st8(f0, vcm, rw, sc0); }
    }
    const v4u d2 = pack8<0>(o1); pub(2, d2, sd2);
    const v4u d3 = pack8<1>(o1); pub(3, d3, sd3);
    f0 = row8(fold(d2)); f1 = row8(fold(d3)); f0 = half8(col < 8 ? f0 : f1); f0.x += tag; st8(f0, vcm, rw, sc1);
    STAMP(58);
    if (g == 0) {
      const u32 sg = b * SLAB + IN9, vcl = row * 32;
      const rsrc_t ry = __builtin_amdgcn_make_buffer_rsrc((void*)out, 0, 512 * 1152, 0x00020000);
      const u32 one = (col & 1) ? 0x3F800000u : 0x00003F80u;
      v4u sel = {(col >> 1) == 0 ? one : 0u, (col >> 1) == 1 ? one : 0u, (col >> 1) == 2 ? one : 0u, (col >> 1) == 3 ? one : 0u};
      u32 vy = col < 8 ? (64 * bp + 32 * (hq >> 2) + 16 * ((hq >> 1) & 1) + (col & 3) + 8 * ((col >> 2) & 1) + 4 * (i >> 1)) * 1152 + 8 * row + (512 + 32 * (hq & 1) + 16 * (i & 1)) * 2 : OOB;
      PIN(sel); PIN(vy);
      asm volatile("s_waitcnt vmcnt(%0) lgkmcnt(0)\n\ts_barrier" :: "i"(TSYNC - 1) : "memory");
      v4u d[8]; v2u c[8];
      __builtin_amdgcn_s_sleep(GSLEEP3);
      _Pragma("nounroll") for (int sp = 0; sp < SPIN_CAP; ++sp) {
        asm volatile("buffer_load_dwordx4 %0, %16, %18, %19 offen sc1\n\tbuffer_load_dwordx4 %1, %16, %18, %19 offen offset:1024 sc1\n\t"
                     "buffer_load_dwordx4 %2, %16, %18, %19 offen offset:2048 sc1\n\tbuffer_load_dwordx4 %3, %16, %18, %19 offen offset:3072 sc1\n\t"
                     "buffer_load_dwordx4 %4, %16, %18, %20 offen sc1\n\tbuffer_load_dwordx4 %5, %16, %18, %20 offen offset:1024 sc1\n\t"
                     "buffer_load_dwordx4 %6, %16, %18, %20 offen offset:2048 sc1\n\tbuffer_load_dwordx4 %7, %16, %18, %20 offen offset:3072 sc1\n\t"
                     "buffer_load_dwordx2 %8, %17, %18, %21 offen sc1\n\tbuffer_load_dwordx2 %9, %17, %18, %21 offen offset:128 sc1\n\t"
                     "buffer_load_dwordx2 %10, %17, %18, %21 offen offset:256 sc1\n\tbuffer_load_dwordx2 %11, %17, %18, %21 offen offset:384 sc1\n\t"
                     "buffer_load_dwordx2 %12, %17, %18, %21 offen offset:512 sc1\n\tbuffer_load_dwordx2 %13, %17, %18, %21 offen offset:640 sc1\n\t"
                     "buffer_load_dwordx2 %14, %17, %18, %21 offen offset:768 sc1\n\tbuffer_load_dwordx2 %15, %17, %18, %21 offen offset:896 sc1\n\t"
                     "s_waitcnt vmcnt(0)"
                     : "=&v"(d[0]), "=&v"(d[1]), "=&v"(d[2]), "=&v"(d[3]), "=&v"(d[4]), "=&v"(d[5]), "=&v"(d[6]), "=&v"(d[7]),
                       "=&v"(c[0]), "=&v"(c[1]), "=&v"(c[2]), "=&v"(c[3]), "=&v"(c[4]), "=&v"(c[5]), "=&v"(c[6]), "=&v"(c[7])
                     : "v"(v16), "v"(vcl), "s"(rw), "s"(sg), "s"(sg + 4096), "s"(sg + (C9 - IN9)) : "memory");
        const v2u f0 = fold(d[3], fold(d[2], fold(d[1], fold(d[0])))), f1 = fold(d[7], fold(d[6], fold(d[5], fold(d[4]))));
        const v2u f = half8(row8(v2u{f0.x + f1.x, f0.y ^ f1.y}));
        u32 cs = 0, cx = 0;
#pragma unroll
        for (int n = 0; n < 8; ++n) { cs += c[n].x; cx ^= c[n].y; }
        if (__all(f.x + 8 * tag == cs && f.y == cx)) break;
        __builtin_amdgcn_s_sleep(2);
      }
      f32x4 y = {0.f, 0.f, 0.f, 0.f};
#pragma unroll
      for (int n = 0; n < 8; ++n) y = MF16(BC(d[n]), BC(sel), y);
      __builtin_amdgcn_raw_buffer_store_b64(v2u{pk(y[0], y[1]), pk(y[2], y[3])}, ry, vy, 0, 0);
      if (me == 0) __builtin_amdgcn_raw_buffer_store_b32(tag, rw, lane == 0 ? 0u : OOB, bp * (32 * SLAB) + EPOCH, 16);
    }
    else asm volatile("s_waitcnt vmcnt(%0) lgkmcnt(0)\n\ts_barrier" :: "i"(TSYNC - 1) : "memory");
  }
  STAMP(60);
}

extern "C" void kernel_launch(void* const* d_in, const int* in_sizes, int n_in, void* d_out, int out_size, void* d_ws, size_t ws_size,
                              hipStream_t stream) {
  gqa_fused<<<256, 512, 0, stream>>>(d_in[0], d_in[1], d_in[2], d_in[3], d_in[4], d_out, (char*)d_ws + WSOFF, (char*)d_ws
  );
}
```
